```python
import torch

def _softmax(x, axis=-1):
    return torch.softmax(x, dim=axis)

def _gelu(x):
    return torch.nn.functional.gelu(x, approximate='tanh')

def _silu(x):
    return torch.nn.functional.silu(x)

def _sigmoid(x):
    return torch.sigmoid(x)

def _layernorm(x, axis=-1, eps=1e-05):
    m = x.mean(dim=axis, keepdim=True)
    v = x.var(dim=axis, keepdim=True, unbiased=False)
    return (x - m) / torch.sqrt(v + eps)

def _scatter_add(tgt, idx, src, axis=0):
    return tgt.clone().index_add_(axis, idx, src)

def _segment_sum(src, seg, n):
    out = torch.zeros((n,) + src.shape[1:], dtype=src.dtype)
    return out.index_add_(0, seg, src)

def _pad(x, pw):
    flat = tuple((v for pair in reversed(pw) for v in pair))
    return torch.nn.functional.pad(x, flat)
kernel_name = 'gated_mix__mha_block__mixer_gmlp__prompt'

def setup_inputs():

    _w = lambda a: (lambda t: t.to(torch.bfloat16) if t.is_floating_point() else t)(torch.as_tensor(a))

    def _A_setup():
        B, S, D, H = (1, 2048, 512, 32)
        return {'x': _w((lambda *s: torch.randn(*s, dtype=torch.bfloat16) * 0.05)(B, S, D)), 'wqkv': _w((lambda *s: torch.randn(*s, dtype=torch.bfloat16) * 1.6)(D, 3 * D)), 'wo': _w((lambda *s: torch.randn(*s, dtype=torch.bfloat16) * 0.1)(D, D))}
    _A_in = _A_setup()

    def _B_setup():
        B, S, D, F = (1, 2048, 512, 1792)
        Fh = F // 2
        return {'x': _w((lambda *s: torch.randn(*s, dtype=torch.bfloat16) * 0.05)(B, S, D)), 'w_in': _w((lambda *s: torch.randn(*s, dtype=torch.bfloat16) * 0.8)(D, 2 * Fh)), 'w_sp': _w(0.01 * (lambda *s: torch.randn(*s, dtype=torch.bfloat16) * 0.8)(S, S)), 'b_sp': _w(1.0 + 0.01 * (lambda *s: torch.randn(*s, dtype=torch.bfloat16) * 0.05)(S)), 'w_out': _w((lambda *s: torch.randn(*s, dtype=torch.bfloat16) * 0.05)(Fh, D))}
    _B_in = _B_setup()
    return {'A_x': _A_in['x'], 'A_wqkv': _A_in['wqkv'], 'A_wo': _A_in['wo'], 'B_x': _B_in['x'], 'B_w_in': _B_in['w_in'], 'B_w_sp': _B_in['w_sp'], 'B_b_sp': _B_in['b_sp'], 'B_w_out': _B_in['w_out'], 'gate_w': _w((lambda *s: torch.randn(*s, dtype=torch.bfloat16) * 0.05)(512) * 16.0)}

def reference(A_x, A_wqkv, A_wo, B_x, B_w_in, B_w_sp, B_b_sp, B_w_out, gate_w):

    def _A_ref(x, wqkv, wo):
        B, S, D = x.shape
        H = 32
        Dh = D // H
        qkv = (lambda x, *d: x.permute(*d))((x @ wqkv).reshape(B, S, 3, H, Dh), 2, 0, 3, 1, 4)
        q, k, v = (qkv[0], qkv[1], qkv[2])
        scores = torch.einsum('bhqd,bhkd->bhqk', q, k) * Dh ** (-0.5)
        p = _softmax(scores, axis=-1)
        o = torch.einsum('bhqk,bhkd->bhqd', p, v)
        return (lambda x, *d: x.permute(*d))(o, 0, 2, 1, 3).reshape(B, S, D) @ wo

    def _B_ref(x, w_in, w_sp, b_sp, w_out):
        B, S, D = x.shape
        F = w_in.shape[-1]
        Fh = F // 2
        z = _gelu(torch.einsum('bsd,df->bsf', x, w_in))
        z1 = z[..., :Fh].clone()
        z2 = _layernorm(z[..., Fh:], axis=-1)
        g = torch.einsum('ts,bsf->btf', w_sp, z2) + b_sp[None, :, None]
        return torch.einsum('bsf,fd->bsd', z1 * g, w_out)
    x = A_x
    g = _sigmoid((x * gate_w).sum(-1, keepdims=True))
    a = _A_ref(x, A_wqkv, A_wo)
    b = _B_ref(x, B_w_in, B_w_sp, B_b_sp, B_w_out)
    return g * a + (1 - g) * b
```

```cpp
#include <hip/hip_runtime.h>
#include <hip/hip_bf16.h>
#include <stdint.h>

typedef __attribute__((ext_vector_type(8))) short bf16x8;
typedef __attribute__((ext_vector_type(4))) short s16x4;
typedef __attribute__((ext_vector_type(16))) float f32x16;
typedef __attribute__((ext_vector_type(2))) float f32x2;
typedef __attribute__((ext_vector_type(4))) unsigned u32x4;
typedef __attribute__((ext_vector_type(2))) unsigned u32x2;
typedef __attribute__((ext_vector_type(2))) __bf16 bf2_t;
#define LDSAS __attribute__((address_space(3)))
typedef LDSAS const char* lds_cptr;
typedef LDSAS char* lds_ptr;
typedef unsigned short u16;
#define DI __device__ __forceinline__
#define MFMA(a, b, c) __builtin_amdgcn_mfma_f32_32x32x16_bf16((a), (b), (c), 0, 0, 0)

constexpr int S_ = 2048, D_ = 512, H_ = 32, FH = 896;
constexpr float QSCALE = 0.25f * 1.4426950408889634f;

constexpr size_t WS_Q = 0;
constexpr size_t WS_K = WS_Q + (size_t)S_ * D_ * 2;
constexpr size_t WS_V = WS_K + (size_t)S_ * D_ * 2;
constexpr size_t WS_Z1 = WS_V + (size_t)S_ * D_ * 2;
constexpr size_t WS_Z2 = WS_Z1 + (size_t)S_ * FH * 2;
constexpr size_t WS_Z2N = WS_Z2 + (size_t)S_ * FH * 2;
constexpr size_t WS_O = WS_Z2N + (size_t)S_ * FH * 2;
constexpr size_t WS_U = WS_O + (size_t)S_ * D_ * 2;
constexpr size_t WS_G = WS_U + (size_t)S_ * FH * 2;

DI unsigned pack2(float lo, float hi) { f32x2 v = {lo, hi}; bf2_t b = __builtin_convertvector(v, bf2_t); return __builtin_bit_cast(unsigned, b); }
DI float bf_lo(unsigned u) { return __uint_as_float(u << 16); }
DI float bf_hi(unsigned u) { return __uint_as_float(u & 0xffff0000u); }

DI void glds16(const void* sbase, unsigned voff, unsigned lds_base) {
    unsigned sv;
    asm volatile("s_mov_b32 %0, m0\n\ts_mov_b32 m0, %3\n\ts_nop 0\n\tglobal_load_lds_dwordx4 %1, %2\n\ts_mov_b32 m0, %0"
                 : "=&s"(sv) : "v"(voff), "s"(sbase), "s"(lds_base) : "memory");
}
#define WAIT_BAR(N) asm volatile("s_waitcnt vmcnt(" #N ") lgkmcnt(0)\n\ts_barrier" ::: "memory")
template <int N> DI void wait_bar() {
    if constexpr (N == 0) WAIT_BAR(0); else if constexpr (N == 2) WAIT_BAR(2); else if constexpr (N == 4) WAIT_BAR(4);
    else if constexpr (N == 6) WAIT_BAR(6); else if constexpr (N == 8) WAIT_BAR(8); else if constexpr (N == 12) WAIT_BAR(12);
    else if constexpr (N == 16) WAIT_BAR(16); else if constexpr (N == 18) WAIT_BAR(18); else if constexpr (N == 20) WAIT_BAR(20);
    else static_assert(N < 0, "add wait count");
}
typedef short v4i16_t __attribute__((ext_vector_type(4)));
DI bf16x8 lds_b128(lds_cptr p) { return *(const LDSAS bf16x8*)p; }
DI s16x4 lds_tr(lds_cptr p) { return __builtin_bit_cast(s16x4, __builtin_amdgcn_ds_read_tr16_b64_v4i16((LDSAS v4i16_t*)p)); }
DI bf16x8 lds_tr2(lds_cptr p, int second_off) { s16x4 lo = lds_tr(p), hi = lds_tr(p + second_off); return __builtin_shufflevector(lo, hi, 0, 1, 2, 3, 4, 5, 6, 7); }

DI float gelu_tanh(float x) {
    const float c1 = -1.5957691216057308f * 1.4426950408889634f, c2 = c1 * 0.044715f;
    float t = x * __builtin_fmaf(x * x, c2, c1);
    float e = __builtin_amdgcn_exp2f(t);
    return x * __builtin_amdgcn_rcpf(1.f + e);
}

constexpr int K1_XB = 128 * 128;
constexpr int K1_WP = 448;
constexpr int K1_WB = 64 * K1_WP;
constexpr int K1_STAGE = K1_XB + K1_WB;
constexpr int K1_LDS = 3 * K1_STAGE;

template <int NS>
DI void k1_run(lds_cptr smem, unsigned lds0, const char* xbase, const char* wbase, unsigned wstep, const unsigned (&xoff)[2], const unsigned (&woff)[4],
               const bool (&wact)[4], int wid, int lane, int wt, int sub0, f32x16 (&acc)[4]) {
    const int r = lane & 31, h = lane >> 5;
    auto issue = [&](int kt, int buf) {
        const char* xb = xbase + kt * 128;
        const char* wb = wbase + (size_t)kt * wstep;
        const unsigned lx = lds0 + buf * K1_STAGE + wid * 2048, lw = lds0 + buf * K1_STAGE + K1_XB + wid * 3584;
        glds16(xb, xoff[0], lx); glds16(xb, xoff[1], lx + 1024);
#pragma unroll
        for (int i = 0; i < 4; ++i) if (wact[i]) glds16(wb, woff[i], lw + i * 1024);
    };
    issue(0, 0); issue(1, 1); issue(2, 2);
    const int tok = wt * 32 + r, swz = (tok >> 1) & 7;
    int xa[4];
#pragma unroll
    for (int ks = 0; ks < 4; ++ks) xa[ks] = tok * 128 + (((2 * ks + h) ^ swz) << 4);
    const int i16 = lane & 15, q = i16 >> 2, pp = i16 & 3;
    const int wa = K1_XB + (8 * h + q) * K1_WP + sub0 * 64 + ((lane >> 4) & 1) * 32 + pp * 8;
#pragma unroll
    for (int kt = 0; kt < 8; ++kt) {
        if (kt == 0) wait_bar<12>(); else if (kt < 7) wait_bar<6>(); else wait_bar<0>();
        if (kt >= 1 && kt + 2 < 8) issue(kt + 2, (kt + 2) % 3);
        lds_cptr sb = smem + (kt % 3) * K1_STAGE;
#pragma unroll
        for (int ks = 0; ks < 4; ++ks) {
            const bf16x8 xf = lds_b128(sb + xa[ks]);
#pragma unroll
            for (int s = 0; s < NS; ++s) {
                const bf16x8 wf = lds_tr2(sb + wa + ks * 16 * K1_WP + s * 64, 4 * K1_WP);
                acc[s] = MFMA(wf, xf, acc[s]);
            }
        }
    }
}

__global__ __launch_bounds__(512, 2) void k1_kernel(const u16* __restrict__ x, const u16* __restrict__ wqkv, const u16* __restrict__ win, char* __restrict__ ws) {
    extern __shared__ __attribute__((aligned(16))) char smem_[];
    lds_cptr smem = (lds_cptr)smem_;
    const unsigned lds0 = (unsigned)(size_t)smem;
    const int b = blockIdx.x, xi = b & 7, j = b >> 3;
    const int panel = 8 * (xi & 1) + (j & 7), cg = 4 * (xi >> 1) + (j >> 3);
    const int tid = threadIdx.x, lane = tid & 63, wid = __builtin_amdgcn_readfirstlane(tid >> 6);
    const int wt = wid & 3, wc = wid >> 2;
    const bool isz = cg >= 8;
    const int col0 = isz ? (cg - 8) * 224 : cg * 192;
    const int ldw = isz ? 1792 : 1536;
    const int nch = isz ? 28 : 24;
    const char* xbase = (const char*)(x + (size_t)panel * 128 * D_);
    const char* wbase = (const char*)((isz ? win : wqkv) + col0);
    unsigned xoff[2], woff[4]; bool wact[4];
#pragma unroll
    for (int i = 0; i < 2; ++i) { const int row = (wid * 2 + i) * 8 + (lane >> 3), c = lane & 7; xoff[i] = row * 1024 + ((c ^ ((row >> 1) & 7)) << 4); }
#pragma unroll
    for (int i = 0; i < 4; ++i) { const int qq = wid * 224 + i * 64 + lane, kr = qq / 28, ch = qq - kr * 28; woff[i] = kr * ldw * 2 + ch * 16; wact[i] = ch < nch && (i < 3 || lane < 32); }
    f32x16 acc[4];
#pragma unroll
    for (int s = 0; s < 4; ++s) acc[s] = f32x16{};
    const int sub0 = wc == 0 ? 0 : (isz ? 4 : 3);
    const int ns = (isz && wc == 0) ? 4 : 3;
    if (ns == 4) k1_run<4>(smem, lds0, xbase, wbase, 64 * ldw * 2, xoff, woff, wact, wid, lane, wt, sub0, acc);
    else k1_run<3>(smem, lds0, xbase, wbase, 64 * ldw * 2, xoff, woff, wact, wid, lane, wt, sub0, acc);

    const int r = lane & 31, h = lane >> 5;
    const int tok0 = panel * 128 + wt * 32;
    if (!isz) {
#pragma unroll
        for (int s = 0; s < 3; ++s) {
            const int col = col0 + (sub0 + s) * 32, part = col >> 9, hA = (col & 511) >> 4;
            const float sc = part == 0 ? QSCALE : 1.f;
            char* base = ws + (size_t)part * (S_ * D_ * 2);
            const unsigned lo = part == 2 ? (r * 32 + h * 16) : lane * 16;
#pragma unroll
            for (int hf = 0; hf < 2; ++hf) {
                u32x4 pk;
#pragma unroll
                for (int e = 0; e < 4; ++e) pk[e] = pack2(acc[s][8 * hf + 2 * e] * sc, acc[s][8 * hf + 2 * e + 1] * sc);
                *(u32x4*)(base + ((size_t)(hA + hf) * S_ + tok0) * 32 + lo) = pk;
            }
        }
    } else {
        const bool second = col0 >= FH;
        u16* zb = (u16*)(ws + (second ? WS_Z2 : WS_Z1)) + (size_t)(tok0 + r) * FH + (col0 - (second ? FH : 0)) + sub0 * 32 + 4 * h;
#pragma unroll
        for (int s = 0; s < 4; ++s) {
            if (s < ns) {
#pragma unroll
                for (int g = 0; g < 4; ++g) {
                    u32x2 pk;
                    pk[0] = pack2(gelu_tanh(acc[s][4 * g]), gelu_tanh(acc[s][4 * g + 1]));
                    pk[1] = pack2(gelu_tanh(acc[s][4 * g + 2]), gelu_tanh(acc[s][4 * g + 3]));
                    *(u32x2*)(zb + s * 32 + 8 * g) = pk;
                }
            }
        }
    }
}

constexpr int AT_CH = 16384;
constexpr int AT_NBUF = 4;
constexpr int AT_ONES = AT_NBUF * AT_CH;
constexpr int AT_LDS = AT_ONES + 8192 + 256;

DI float half_max(float m) {
    auto rr = __builtin_amdgcn_permlane32_swap(__float_as_uint(m), __float_as_uint(m), false, false);
    return __builtin_fmaxf(__uint_as_float(rr[0]), __uint_as_float(rr[1]));
}
DI float max16(const f32x16& s) {
    float a = __builtin_fmaxf(s[0], s[1]);
#pragma unroll
    for (int i = 2; i < 16; ++i) a = __builtin_fmaxf(a, s[i]);
    return a;
}
DI void exp_pack(const f32x16& s, bf16x8& p0, bf16x8& p1) {
    u32x4 a, b;
#pragma unroll
    for (int e = 0; e < 4; ++e) a[e] = pack2(__builtin_amdgcn_exp2f(s[2 * e]), __builtin_amdgcn_exp2f(s[2 * e + 1]));
#pragma unroll
    for (int e = 0; e < 4; ++e) b[e] = pack2(__builtin_amdgcn_exp2f(s[8 + 2 * e]), __builtin_amdgcn_exp2f(s[8 + 2 * e + 1]));
    p0 = __builtin_bit_cast(bf16x8, a); p1 = __builtin_bit_cast(bf16x8, b);
}

DI void attn_safe(const char* Kh, const char* Vh, bf16x8 qf, int lane, f32x16& Oout) {
    const int r = lane & 31, h = lane >> 5;
    f32x16 O = f32x16{}; float m = -INFINITY;
    const f32x16 zero = f32x16{};
    for (int t = 0; t < S_ / 32; ++t) {
        const bf16x8 kf = *(const bf16x8*)(Kh + t * 1024 + lane * 16);
        f32x16 s = MFMA(kf, qf, zero);
        const float mn = __builtin_fmaxf(m, half_max(max16(s)));
        const float al = __builtin_amdgcn_exp2f(m - mn);
        m = mn;
#pragma unroll
        for (int i = 0; i < 16; ++i) { O[i] *= al; s[i] -= mn; }
        bf16x8 p0, p1; exp_pack(s, p0, p1);
        bf16x8 v0, v1;
#pragma unroll
        for (int jj = 0; jj < 8; ++jj) {
            const int key = t * 32 + 8 * (jj >> 2) + 4 * h + (jj & 3);
            v0[jj] = r < 16 ? *(const short*)(Vh + key * 32 + r * 2) : (short)0x3f80;
            v1[jj] = r < 16 ? *(const short*)(Vh + (key + 16) * 32 + r * 2) : (short)0x3f80;
        }
        O = MFMA(v0, p0, O); O = MFMA(v1, p1, O);
    }
    Oout = O;
}

template <int NW>
__global__ __launch_bounds__(NW * 64) void attn_kernel(const char* __restrict__ ws_in, char* __restrict__ ws) {
    constexpr int QB = 8 / NW, PPW = 16 / NW;
    extern __shared__ __attribute__((aligned(16))) char smem_[];
    lds_cptr smem = (lds_cptr)smem_;
    const unsigned lds0 = (unsigned)(size_t)smem;
    const int b = blockIdx.x, head = (b & 7) * 4 + ((b >> 3) & 3), qr = b >> 5;
    const int tid = threadIdx.x, lane = tid & 63, wid = __builtin_amdgcn_readfirstlane(tid >> 6);
    const int r = lane & 31, h = lane >> 5;
    const char* Kh = ws_in + WS_K + (size_t)head * S_ * 32;
    const char* Vh = ws_in + WS_V + (size_t)head * S_ * 32;
    const char* Qh = ws_in + WS_Q + ((size_t)head * S_ + qr * 256 + wid * QB * 32) * 32;
    auto issue = [&](int c) {
        const unsigned dst = lds0 + (c & 3) * AT_CH;
#pragma unroll
        for (int i = 0; i < PPW; ++i) {
            const int p = wid * PPW + i;
            const char* src = (p < 8 ? Kh : Vh) + c * 8192;
            glds16(src, (unsigned)((p & 7) * 1024 + lane * 16), dst + p * 1024);
        }
    };
    issue(0); issue(1); issue(2);
    for (int i = tid; i < (8192 + 256) / 16; i += NW * 64) *(LDSAS u32x4*)((lds_ptr)smem_ + AT_ONES + i * 16) = u32x4{0x3f803f80u, 0x3f803f80u, 0x3f803f80u, 0x3f803f80u};
    bf16x8 qf[QB];
#pragma unroll
    for (int i = 0; i < QB; ++i) qf[i] = *(const bf16x8*)(Qh + i * 1024 + lane * 16);
    const int i16 = lane & 15, q4 = i16 >> 2, pp = i16 & 3;
    const bool isv = (lane & 16) == 0;
    const int va_lane = isv ? (8192 + (4 * h + q4) * 32 + pp * 8) : (AT_ONES + (h ? 0 : 128));
    const int ka_lane = lane * 16;

    f32x16 O[QB], C[QB], Sa[QB], Sb[QB];
    wait_bar<0>();
    {
        const bf16x8 kf = lds_b128(smem + ka_lane);
#pragma unroll
        for (int i = 0; i < QB; ++i) {
            const f32x16 s0 = MFMA(kf, qf[i], f32x16{});
            const float m = half_max(max16(s0));
#pragma unroll
            for (int e = 0; e < 16; ++e) { C[i][e] = -m; O[i][e] = 0.f; }
            Sa[i] = MFMA(kf, qf[i], C[i]);
        }
    }
    for (int c = 0; c < 8; ++c) {
        const int buf = (c & 3) * AT_CH;
        lds_cptr kb = smem + buf + ka_lane;
        lds_cptr vb = smem + (isv ? buf : 0) + va_lane;
        lds_cptr kn = smem + ((c + 1) & 3) * AT_CH + ka_lane;
#pragma unroll
        for (int j = 0; j < 8; ++j) {
            if (j == 7) {
                if (c < 6) wait_bar<PPW>(); else wait_bar<0>();
                if (c + 3 < 8) issue(c + 3);
            }
            const bf16x8 kf = (j < 7) ? lds_b128(kb + (j + 1) * 1024) : lds_b128(kn);
            const bf16x8 v0 = lds_tr2(vb + j * 1024, 256), v1 = lds_tr2(vb + j * 1024 + 512, 256);
#pragma unroll
            for (int i = 0; i < QB; ++i) {
                f32x16& cur = (j & 1) ? Sb[i] : Sa[i];
                f32x16& nxt = (j & 1) ? Sa[i] : Sb[i];
                nxt = MFMA(kf, qf[i], C[i]);
                bf16x8 p0, p1; exp_pack(cur, p0, p1);
                O[i] = MFMA(v0, p0, O[i]); O[i] = MFMA(v1, p1, O[i]);
            }
        }
    }
#pragma unroll
    for (int i = 0; i < QB; ++i) {
        float l = O[i][8];
        bool bad = !(l < 1e30f) || !(l > 1e-30f);
#pragma unroll
        for (int e = 0; e < 8; ++e) bad |= !(__builtin_fabsf(O[i][e]) < 3e38f);
        if (__any(bad)) { attn_safe(Kh, Vh, qf[i], lane, O[i]); l = O[i][8]; }
        const float inv = 1.f / l;
        u32x4 pk;
#pragma unroll
        for (int e = 0; e < 4; ++e) pk[e] = pack2(O[i][2 * e] * inv, O[i][2 * e + 1] * inv);
        const int t = qr * 256 + (wid * QB + i) * 32 + r;
        *(u32x4*)(ws + WS_O + (size_t)t * 1024 + head * 32 + h * 16) = pk;
    }
}

__global__ __launch_bounds__(256) void norm_kernel(const u16* __restrict__ x, const u16* __restrict__ gw, char* __restrict__ ws) {
    const int lane = threadIdx.x & 63, row = blockIdx.x * 4 + (threadIdx.x >> 6);
    const unsigned* src = (const unsigned*)(ws + WS_Z2 + (size_t)row * FH * 2);
    unsigned v[7]; float s = 0.f;
#pragma unroll
    for (int i = 0; i < 7; ++i) { v[i] = src[i * 64 + lane]; s += bf_lo(v[i]) + bf_hi(v[i]); }
    const u32x4 xv = *(const u32x4*)(x + (size_t)row * D_ + lane * 8), gv = *(const u32x4*)(gw + lane * 8);
    float gs = 0.f;
#pragma unroll
    for (int i = 0; i < 4; ++i) gs += bf_lo(xv[i]) * bf_lo(gv[i]) + bf_hi(xv[i]) * bf_hi(gv[i]);
#pragma unroll
    for (int o = 32; o > 0; o >>= 1) { s += __shfl_xor(s, o); gs += __shfl_xor(gs, o); }
    const float mean = s * (1.f / FH);
    float q = 0.f;
#pragma unroll
    for (int i = 0; i < 7; ++i) { const float a = bf_lo(v[i]) - mean, c = bf_hi(v[i]) - mean; q += a * a + c * c; }
#pragma unroll
    for (int o = 32; o > 0; o >>= 1) q += __shfl_xor(q, o);
    const float rstd = 1.f / __builtin_sqrtf(q * (1.f / FH) + 1e-5f);
    unsigned* dst = (unsigned*)(ws + WS_Z2N + (size_t)row * FH * 2);
#pragma unroll
    for (int i = 0; i < 7; ++i) dst[i * 64 + lane] = pack2((bf_lo(v[i]) - mean) * rstd, (bf_hi(v[i]) - mean) * rstd);
    if (lane == 0) ((float*)(ws + WS_G))[row] = 1.f / (1.f + __expf(-gs));
}

constexpr int SP_AB = 128 * 128, SP_ZB = 64 * 128, SP_STAGE = SP_AB + SP_ZB, SP_LDS = 3 * SP_STAGE;
__global__ __launch_bounds__(256) void spatial_kernel(const u16* __restrict__ wsp, const u16* __restrict__ bsp, char* __restrict__ ws) {
    extern __shared__ __attribute__((aligned(16))) char smem_[];
    lds_cptr smem = (lds_cptr)smem_;
    const unsigned lds0 = (unsigned)(size_t)smem;
    const int b = blockIdx.x, xi = b & 7, j = b >> 3;
    if (j >= 28) return;
    const int tt = 4 * (xi & 3) + (j & 3), ft = 7 * (xi >> 2) + (j >> 2);
    const int tid = threadIdx.x, lane = tid & 63, wid = __builtin_amdgcn_readfirstlane(tid >> 6);
    const int r = lane & 31, h = lane >> 5, wt = wid & 1, wf = wid >> 1;
    const char* abase = (const char*)(wsp + (size_t)tt * 128 * S_);
    const char* zbase = ws + WS_Z2N + ft * 128;
    unsigned aoff[4], zoff[2];
#pragma unroll
    for (int i = 0; i < 4; ++i) { const int row = (wid * 4 + i) * 8 + (lane >> 3), c = lane & 7; aoff[i] = row * (S_ * 2) + ((c ^ ((row >> 1) & 7)) << 4); }
#pragma unroll
    for (int i = 0; i < 2; ++i) { const int kr = (wid * 2 + i) * 8 + (lane >> 3), c = lane & 7; zoff[i] = kr * (FH * 2) + ((c ^ (((kr >> 1) & 1) << 2)) << 4); }
    auto issue = [&](int kt, int buf) {
        const char* ab = abase + kt * 128;
        const char* zb = zbase + (size_t)kt * 64 * FH * 2;
        const unsigned la = lds0 + buf * SP_STAGE + wid * 4096, lz = lds0 + buf * SP_STAGE + SP_AB + wid * 2048;
#pragma unroll
        for (int i = 0; i < 4; ++i) glds16(ab, aoff[i], la + i * 1024);
#pragma unroll
        for (int i = 0; i < 2; ++i) glds16(zb, zoff[i], lz + i * 1024);
    };
    issue(0, 0); issue(1, 1); issue(2, 2);
    const int swz = (r >> 1) & 7;
    int aa[4];
#pragma unroll
    for (int ks = 0; ks < 4; ++ks) aa[ks] = (wt * 64 + r) * 128 + (((2 * ks + h) ^ swz) << 4);
    const int i16 = lane & 15, q = i16 >> 2, pp = i16 & 3;
    const int zch = (4 * wf + 2 * ((lane >> 4) & 1) + (pp >> 1)) ^ (((q >> 1) & 1) << 2);
    const int za = SP_AB + (8 * h + q) * 128 + zch * 16 + (pp & 1) * 8;
    f32x16 acc[2] = {f32x16{}, f32x16{}};
    int buf = 0;
    for (int kt = 0; kt < 32; ++kt) {
        if (kt == 0) wait_bar<12>(); else if (kt < 31) wait_bar<6>(); else wait_bar<0>();
        if (kt >= 1 && kt + 2 < 32) { int nb = buf + 2; if (nb >= 3) nb -= 3; issue(kt + 2, nb); }
        lds_cptr sb = smem + buf * SP_STAGE;
#pragma unroll
        for (int ks = 0; ks < 4; ++ks) {
            const bf16x8 zf = lds_tr2(sb + za + ks * 2048, 512);
            const bf16x8 a0 = lds_b128(sb + aa[ks]), a1 = lds_b128(sb + aa[ks] + 4096);
            acc[0] = MFMA(zf, a0, acc[0]);
            acc[1] = MFMA(zf, a1, acc[1]);
        }
        buf = buf == 2 ? 0 : buf + 1;
    }
#pragma unroll
    for (int i = 0; i < 2; ++i) {
        const int t = tt * 128 + wt * 64 + 32 * i + r;
        const float bias = __uint_as_float((unsigned)bsp[t] << 16);
        const size_t off = (size_t)t * FH + ft * 64 + wf * 32 + 4 * h;
        const u16* z1 = (const u16*)(ws + WS_Z1) + off;
        u16* u = (u16*)(ws + WS_U) + off;
#pragma unroll
        for (int g = 0; g < 4; ++g) {
            const u32x2 zv = *(const u32x2*)(z1 + 8 * g);
            u32x2 pk;
            pk[0] = pack2(bf_lo(zv[0]) * (acc[i][4 * g] + bias), bf_hi(zv[0]) * (acc[i][4 * g + 1] + bias));
            pk[1] = pack2(bf_lo(zv[1]) * (acc[i][4 * g + 2] + bias), bf_hi(zv[1]) * (acc[i][4 * g + 3] + bias));
            *(u32x2*)(u + 8 * g) = pk;
        }
    }
}

constexpr int F_STAGE = 16384, F_NST = 4, F_LDS = F_NST * F_STAGE;
__global__ __launch_bounds__(256) void final_kernel(const u16* __restrict__ wo, const u16* __restrict__ wout, const char* __restrict__ ws, u16* __restrict__ out) {
    extern __shared__ __attribute__((aligned(16))) char smem_[];
    lds_cptr smem = (lds_cptr)smem_;
    const unsigned lds0 = (unsigned)(size_t)smem;
    const int b = blockIdx.x, xi = b & 7, j = b >> 3;
    const int tt = 4 * xi + (j & 3), nt = j >> 2;
    const int tid = threadIdx.x, lane = tid & 63, wid = __builtin_amdgcn_readfirstlane(tid >> 6);
    const int r = lane & 31, h = lane >> 5, wt = wid & 1, wn = wid >> 1;
    const char* obase = ws + WS_O + (size_t)tt * 64 * 1024;
    const char* ubase = ws + WS_U + (size_t)tt * 64 * FH * 2;
    unsigned ooff[2], uoff[2], woff[2];
#pragma unroll
    for (int i = 0; i < 2; ++i) {
        const int row = (wid * 2 + i) * 8 + (lane >> 3), c = lane & 7;
        const int gc = (c ^ ((row >> 1) & 7)) << 4;
        ooff[i] = row * 1024 + gc; uoff[i] = row * (FH * 2) + gc;
        woff[i] = row * 1024 + ((c ^ (((row >> 1) & 1) << 2)) << 4);
    }
    auto issue = [&](int kt, int buf) {
        const unsigned la = lds0 + buf * F_STAGE + wid * 2048, lw = la + 8192;
        if (kt < 8) {
            const char* ab = obase + kt * 128; const char* wb = (const char*)wo + (size_t)kt * 65536 + nt * 128;
            glds16(ab, ooff[0], la); glds16(ab, ooff[1], la + 1024); glds16(wb, woff[0], lw); glds16(wb, woff[1], lw + 1024);
        } else {
            const char* ab = ubase + (kt - 8) * 128; const char* wb = (const char*)wout + (size_t)(kt - 8) * 65536 + nt * 128;
            glds16(ab, uoff[0], la); glds16(ab, uoff[1], la + 1024); glds16(wb, woff[0], lw); glds16(wb, woff[1], lw + 1024);
        }
    };
    issue(0, 0); issue(1, 1); issue(2, 2);
    const int swz = (r >> 1) & 7;
    int aa[4];
#pragma unroll
    for (int ks = 0; ks < 4; ++ks) aa[ks] = (wt * 32 + r) * 128 + (((2 * ks + h) ^ swz) << 4);
    const int i16 = lane & 15, q = i16 >> 2, pp = i16 & 3;
    const int wch = (4 * wn + 2 * ((lane >> 4) & 1) + (pp >> 1)) ^ (((q >> 1) & 1) << 2);
    const int wa = 8192 + (8 * h + q) * 128 + wch * 16 + (pp & 1) * 8;
    f32x16 accA = f32x16{}, accB = f32x16{};
    for (int kt = 0; kt < 22; ++kt) {
        if (kt < 20) wait_bar<8>(); else if (kt == 20) wait_bar<4>(); else wait_bar<0>();
        if (kt + 3 < 22) issue(kt + 3, (kt + 3) & 3);
        lds_cptr sb = smem + (kt & 3) * F_STAGE;
        if (kt < 8) {
#pragma unroll
            for (int ks = 0; ks < 4; ++ks) accA = MFMA(lds_tr2(sb + wa + ks * 2048, 512), lds_b128(sb + aa[ks]), accA);
        } else {
#pragma unroll
            for (int ks = 0; ks < 4; ++ks) accB = MFMA(lds_tr2(sb + wa + ks * 2048, 512), lds_b128(sb + aa[ks]), accB);
        }
    }
    const int t = tt * 64 + wt * 32 + r;
    const float g = ((const float*)(ws + WS_G))[t], g1 = 1.f - g;
    u16* op = out + (size_t)t * D_ + nt * 64 + wn * 32 + 4 * h;
#pragma unroll
    for (int gg = 0; gg < 4; ++gg) {
        u32x2 pk;
        pk[0] = pack2(g * accA[4 * gg] + g1 * accB[4 * gg], g * accA[4 * gg + 1] + g1 * accB[4 * gg + 1]);
        pk[1] = pack2(g * accA[4 * gg + 2] + g1 * accB[4 * gg + 2], g * accA[4 * gg + 3] + g1 * accB[4 * gg + 3]);
        *(u32x2*)(op + 8 * gg) = pk;
    }
}

extern "C" void kernel_launch(void* const* d_in, const int* in_sizes, int n_in, void* d_out, int out_size, void* d_ws, size_t ws_size, hipStream_t stream) {
    const u16* x = (const u16*)d_in[0];
    const u16* wqkv = (const u16*)d_in[1];
    const u16* wo = (const u16*)d_in[2];
    const u16* win = (const u16*)d_in[4];
    const u16* wsp = (const u16*)d_in[5];
    const u16* bsp = (const u16*)d_in[6];
    const u16* wout = (const u16*)d_in[7];
    const u16* gw = (const u16*)d_in[8];
    char* ws = (char*)d_ws;
    (void)hipFuncSetAttribute((const void*)k1_kernel, hipFuncAttributeMaxDynamicSharedMemorySize, K1_LDS);
    (void)hipFuncSetAttribute((const void*)attn_kernel<8>, hipFuncAttributeMaxDynamicSharedMemorySize, AT_LDS);
    (void)hipFuncSetAttribute((const void*)spatial_kernel, hipFuncAttributeMaxDynamicSharedMemorySize, SP_LDS);
    (void)hipFuncSetAttribute((const void*)final_kernel, hipFuncAttributeMaxDynamicSharedMemorySize, F_LDS);
    k1_kernel<<<256, 512, K1_LDS, stream>>>(x, wqkv, win, ws);
    attn_kernel<8><<<256, 512, AT_LDS, stream>>>(ws, ws);
    norm_kernel<<<512, 256, 0, stream>>>(x, gw, ws);
    spatial_kernel<<<256, 256, SP_LDS, stream>>>(wsp, bsp, ws);
    final_kernel<<<256, 256, F_LDS, stream>>>(wo, wout, ws, (u16*)d_out);
}
```

```cpp
#include <hip/hip_runtime.h>
#include <hip/hip_bf16.h>
#include <stdint.h>
#include <type_traits>

typedef __attribute__((ext_vector_type(8))) short bf16x8;
typedef __attribute__((ext_vector_type(4))) short s16x4;
typedef __attribute__((ext_vector_type(16))) float f32x16;
typedef __attribute__((ext_vector_type(2))) float f32x2;
typedef __attribute__((ext_vector_type(4))) unsigned u32x4;
typedef __attribute__((ext_vector_type(2))) unsigned u32x2;
typedef __attribute__((ext_vector_type(2))) __bf16 bf2_t;
#define LDSAS __attribute__((address_space(3)))
typedef LDSAS const char* lds_cptr;
typedef LDSAS char* lds_ptr;
typedef unsigned short u16;
#define DI __device__ __forceinline__
#define MFMA(a, b, c) __builtin_amdgcn_mfma_f32_32x32x16_bf16((a), (b), (c), 0, 0, 0)

constexpr int S_ = 2048, D_ = 512, H_ = 32, FH = 896;
constexpr float QSCALE = 0.25f * 1.4426950408889634f;

constexpr size_t WS_Q = 0;
constexpr size_t WS_K = WS_Q + (size_t)S_ * D_ * 2;
constexpr size_t WS_V = WS_K + (size_t)S_ * D_ * 2;
constexpr size_t WS_Z1 = WS_V + (size_t)S_ * D_ * 2;
constexpr size_t WS_Z2 = WS_Z1 + (size_t)S_ * FH * 2;
constexpr size_t WS_Z2N = WS_Z2 + (size_t)S_ * FH * 2;
constexpr size_t WS_O = WS_Z2N + (size_t)S_ * FH * 2;
constexpr size_t WS_U = WS_O + (size_t)S_ * D_ * 2;
constexpr size_t WS_G = WS_U + (size_t)S_ * FH * 2;
constexpr size_t WS_FLAG = WS_G + 8192;
constexpr int FL_CO = 128, FL_CU = 136, FL_N = 152;

DI unsigned pack2(float lo, float hi) { f32x2 v = {lo, hi}; bf2_t b = __builtin_convertvector(v, bf2_t); return __builtin_bit_cast(unsigned, b); }
DI float bf_lo(unsigned u) { return __uint_as_float(u << 16); }
DI float bf_hi(unsigned u) { return __uint_as_float(u & 0xffff0000u); }

DI void glds16(const void* sbase, unsigned voff, unsigned lds_base) {
    asm volatile("s_mov_b32 m0, %2\n\ts_nop 0\n\tglobal_load_lds_dwordx4 %0, %1" :: "v"(voff), "s"(sbase), "s"(lds_base) : "memory");
}
DI void glds16_sc1(const void* sbase, unsigned voff, unsigned lds_base) {
    asm volatile("s_mov_b32 m0, %2\n\ts_nop 0\n\tglobal_load_lds_dwordx4 %0, %1 sc1" :: "v"(voff), "s"(sbase), "s"(lds_base) : "memory");
}
DI void st16_wt(void* p, u32x4 v) {
    asm volatile("global_store_dwordx4 %0, %1, off sc1\n\ts_nop 1" :: "v"(p), "v"(v) : "memory");
}
template <int N> DI void wait_vm() { asm volatile("s_waitcnt vmcnt(%0)" :: "n"(N) : "memory"); }
#define CFENCE() asm volatile("" ::: "memory")
typedef __attribute__((address_space(1))) unsigned gu32;
#define RLX_AGENT __ATOMIC_RELAXED, __HIP_MEMORY_SCOPE_AGENT
#define SBAR() __builtin_amdgcn_sched_barrier(0)
#define WAIT_BAR(N) asm volatile("s_waitcnt vmcnt(" #N ") lgkmcnt(0)\n\ts_barrier" ::: "memory")
template <int N> DI void wait_bar() {
    if constexpr (N == 0) WAIT_BAR(0); else if constexpr (N == 2) WAIT_BAR(2); else if constexpr (N == 4) WAIT_BAR(4);
    else if constexpr (N == 5) WAIT_BAR(5); else if constexpr (N == 10) WAIT_BAR(10); else if constexpr (N == 6) WAIT_BAR(6); else if constexpr (N == 8) WAIT_BAR(8); else if constexpr (N == 12) WAIT_BAR(12); else if constexpr (N == 7) WAIT_BAR(7); else if constexpr (N == 11) WAIT_BAR(11); else if constexpr (N == 13) WAIT_BAR(13); else if constexpr (N == 22) WAIT_BAR(22); else if constexpr (N == 1) WAIT_BAR(1);
    else if constexpr (N == 16) WAIT_BAR(16); else if constexpr (N == 18) WAIT_BAR(18); else if constexpr (N == 20) WAIT_BAR(20);
    else static_assert(N < 0, "add wait count");
}
typedef short v4i16_t __attribute__((ext_vector_type(4)));
DI bf16x8 lds_b128(lds_cptr p) { return *(const LDSAS bf16x8*)p; }
DI s16x4 lds_tr(lds_cptr p) { return __builtin_bit_cast(s16x4, __builtin_amdgcn_ds_read_tr16_b64_v4i16((LDSAS v4i16_t*)p)); }
DI bf16x8 lds_tr2(lds_cptr p, int second_off) { s16x4 lo = lds_tr(p), hi = lds_tr(p + second_off); return __builtin_shufflevector(lo, hi, 0, 1, 2, 3, 4, 5, 6, 7); }


DI float wave_sum(float v) {
#define DPP_ADD(ctrl, rm) v += __uint_as_float(__builtin_amdgcn_update_dpp(0u, __float_as_uint(v), ctrl, rm, 0xf, true))
    DPP_ADD(0x111, 0xf); DPP_ADD(0x112, 0xf); DPP_ADD(0x114, 0xf); DPP_ADD(0x118, 0xf);
    DPP_ADD(0x142, 0xa); DPP_ADD(0x143, 0xc);
#undef DPP_ADD
    return __uint_as_float(__builtin_amdgcn_readlane(__float_as_uint(v), 63));
}

DI float gelu_tanh(float x) {
    const float c1 = -1.5957691216057308f * 1.4426950408889634f, c2 = c1 * 0.044715f;
    float t = x * __builtin_fmaf(x * x, c2, c1);
    float e = __builtin_amdgcn_exp2f(t);
    return x * __builtin_amdgcn_rcpf(1.f + e);
}

constexpr int K1_XB = 128 * 128;
constexpr int K1_AB = 64 * 192;
constexpr int K1_BB = 64 * 256;
constexpr int K1_STAGE = K1_XB + K1_AB + K1_BB;
constexpr int K1_LDS = 3 * K1_STAGE;

DI void glds16m(const void* sbase, unsigned voff, unsigned lds_base, unsigned long long mask) {
    asm volatile("s_mov_b32 m0, %2\n\ts_mov_b64 exec, %3\n\tglobal_load_lds_dwordx4 %0, %1\n\ts_mov_b64 exec, -1"
                 :: "v"(voff), "s"(sbase), "s"(lds_base), "s"(mask) : "memory");
}

DI void l2_touch(const void* sbase, unsigned voff, unsigned lds_base) {
    asm volatile("s_mov_b32 m0, %2\n\ts_nop 0\n\tglobal_load_lds_dword %0, %1" :: "v"(voff), "s"(sbase), "s"(lds_base) : "memory");
}
constexpr int K1_SCR = 3 * K1_STAGE;
constexpr int K1_GW = K1_SCR + 8 * 256;
constexpr int K1_LDS2 = K1_GW + 1024;

__global__ __launch_bounds__(768, 3) void k1_kernel(const u16* __restrict__ x, const u16* __restrict__ wqkv, const u16* __restrict__ win, const u16* __restrict__ gw,
        const u16* __restrict__ wsp, const u16* __restrict__ wo, const u16* __restrict__ wout, char* __restrict__ ws) {
    extern __shared__ __attribute__((aligned(16))) char smem_[];
    lds_cptr smem = (lds_cptr)smem_;
    const unsigned lds0 = (unsigned)(size_t)smem;
    const int b = blockIdx.x, xi = b & 7, j = b >> 3;
    const int panel = 8 * (xi & 1) + (j & 7), cg = 4 * (xi >> 1) + (j >> 3);
    const int tid = threadIdx.x, lane = tid & 63, wid = __builtin_amdgcn_readfirstlane(tid >> 6);
    const int nz = (cg & 1) ? 3 : 4, zs = (cg >> 1) * 7 + (cg & 1) * 4;
    const char* xbase = (const char*)(x + (size_t)panel * 128 * D_);
    if (wid >= 8) {
        const int lw = wid - 8;
        unsigned xoff[4], aoff[3], boff[4];
#pragma unroll
        for (int i = 0; i < 4; ++i) { const int row = (lw * 4 + i) * 8 + (lane >> 3), c = lane & 7; xoff[i] = row * 1024 + ((c ^ ((row >> 1) & 7)) << 4); }
        auto issueX = [&](int kt, int buf) {
#pragma unroll
            for (int i = 0; i < 4; ++i) glds16(xbase + kt * 128, xoff[i], lds0 + buf * K1_STAGE + lw * 4096 + i * 1024);
        };
        issueX(0, 0);
#pragma unroll
        for (int i = 0; i < 3; ++i) { const int qq = (lw * 3 + i) * 64 + lane, kr = qq / 12, ch = qq - kr * 12; aoff[i] = kr * 3072 + (ch >> 2) * 1024 + (ch & 3) * 16; }
        const int blk = ((lane >> 2) & 3) ^ ((lane >> 4) & 3);
#pragma unroll
        for (int i = 0; i < 4; ++i) { const int kr = (lw * 4 + i) * 4 + (lane >> 4); boff[i] = kr * 3584 + blk * 64 + (lane & 3) * 16; }
        const unsigned long long bmask = __ballot(blk < nz);
        const char* abase = (const char*)wqkv + cg * 64;
        const char* bbase = (const char*)win + zs * 64;
        auto issueW = [&](int kt, int buf) {
#pragma unroll
            for (int i = 0; i < 3; ++i) glds16(abase + (size_t)kt * (64 * 3072), aoff[i], lds0 + buf * K1_STAGE + K1_XB + lw * 3072 + i * 1024);
#pragma unroll
            for (int i = 0; i < 4; ++i) glds16m(bbase + (size_t)kt * (64 * 3584), boff[i], lds0 + buf * K1_STAGE + K1_XB + K1_AB + lw * 4096 + i * 1024, bmask);
        };
        issueW(0, 0);
        if (cg == 1 && lw == 0) glds16(gw, lane * 16, lds0 + K1_GW);
        issueX(1, 1); issueW(1, 1);
        issueX(2, 2); issueW(2, 2);
        if (b == 0 && lw * 64 + lane < FL_N) ((unsigned*)(ws + WS_FLAG))[(lw * 64 + lane) * 32] = 0;
        wait_bar<22>();
        {
            const int i = lw * 64 + lane;
            const int li = (cg & 3) * 160 + (i < 160 ? i : 159), row = li / 5, kt = 3 + li - row * 5;
            l2_touch(xbase, row * 1024 + kt * 128, lds0 + K1_SCR + lw * 256);
            if (lw < 2) {
                int a = i; a = a < 120 ? a : 119;
                const int k2 = 192 + (panel & 7) * 40 + a / 3, sg = a % 3;
                l2_touch(wqkv, k2 * 3072 + sg * 1024 + cg * 64, lds0 + K1_SCR + lw * 256);
            } else {
                int a = i - 128; a = a < 120 ? a : 119;
                const int k2 = 192 + (panel & 7) * 40 + a / 3, sg = a % 3;
                const int bo = sg == 2 ? nz * 64 - 4 : sg * 128;
                l2_touch(win, k2 * 3584 + zs * 64 + bo, lds0 + K1_SCR + lw * 256);
            }
        }
        int buf = 0;
        for (int X = 1; X < 8; ++X) {
            if (X == 2) wait_bar<13>(); else if (X < 7) wait_bar<11>(); else wait_bar<2>();
            if (X + 2 < 8) { issueX(X + 2, buf); issueW(X + 2, buf); }
            if (X == 5) {
                l2_touch(wsp, (b * 256 + lw * 64 + lane) * 128, lds0 + K1_SCR + lw * 256);
                const int l0 = b * 44 + lw * 11;
                const int li = l0 + (lane < 11 ? lane : 10);
                if (l0 + 10 < 4096) l2_touch(wo, li * 128, lds0 + K1_SCR + lw * 256);
                else { const int l2 = li - 4096; l2_touch(wout, (l2 < 0 ? 0 : l2) * 128, lds0 + K1_SCR + lw * 256); }
            }
            buf = buf == 2 ? 0 : buf + 1;
        }
        asm volatile("s_barrier" ::: "memory");
        return;
    }
    const int wt = wid & 3, wc = wid >> 2;
    const bool dogate = cg == 1;
    const int r = lane & 31, h = lane >> 5;
    const int tok = wt * 32 + r, swz = (tok >> 1) & 7;
    int xa[4];
#pragma unroll
    for (int ks = 0; ks < 4; ++ks) xa[ks] = tok * 128 + (((2 * ks + h) ^ swz) << 4);
    const int i16 = lane & 15, q4 = i16 >> 2, pp = i16 & 3;
    const int waA = K1_XB + (8 * h + q4) * 192 + ((lane >> 4) & 1) * 32 + pp * 8;
    int waB[4];
#pragma unroll
    for (int s = 0; s < 4; ++s) waB[s] = K1_XB + K1_AB + (8 * h + q4) * 256 + ((s ^ q4) * 64) + ((lane >> 4) & 1) * 32 + pp * 8;
    f32x16 acc[4];
#pragma unroll
    for (int s = 0; s < 4; ++s) acc[s] = f32x16{};
    bf16x8 xf[2], wf[2][4];
    asm volatile("s_barrier" ::: "memory");
#define K1_RA(s, ks) lds_tr2(sb + waA + (s) * 64 + (ks) * 16 * 192, 4 * 192)
#define K1_RB(s, ks) lds_tr2(sb + waB[s] + (ks) * 16 * 256, 4 * 256)
#define K1_RG(kt_, ks) lds_b128(smem + K1_GW + (kt_) * 128 + (ks) * 32 + h * 16)
    const bool third = nz == 4 || dogate;
    auto run = [&](auto WCC) {
        constexpr int WC = decltype(WCC)::value;
        {
            lds_cptr sb = smem;
            xf[0] = lds_b128(sb + xa[0]);
            if (WC == 0) { wf[0][0] = K1_RA(0, 0); wf[0][1] = K1_RA(1, 0); wf[0][2] = K1_RB(0, 0); wf[0][3] = K1_RB(1, 0); }
            else { wf[0][0] = K1_RA(2, 0); wf[0][1] = K1_RB(2, 0); wf[0][2] = dogate ? K1_RG(0, 0) : K1_RB(3, 0); }
        }
        int buf = 0;
#pragma unroll 1
        for (int kt = 0; kt < 8; ++kt) {
            const int nb = buf == 2 ? 0 : buf + 1;
#pragma unroll
            for (int ks = 0; ks < 4; ++ks) {
                const int c = ks & 1, n = c ^ 1;
                if (ks == 3) asm volatile("s_waitcnt lgkmcnt(0)\n\ts_barrier" ::: "memory");
                lds_cptr sb = smem + (ks < 3 ? buf : nb) * K1_STAGE;
                const int kn = (ks + 1) & 3, kt_r = ks < 3 ? kt : (kt + 1) & 7;
                SBAR();
                if (WC == 0) {
                    acc[0] = MFMA(wf[c][0], xf[c], acc[0]); SBAR(); xf[n] = lds_b128(sb + xa[kn]); wf[n][0] = K1_RA(0, kn); SBAR();
                    acc[1] = MFMA(wf[c][1], xf[c], acc[1]); SBAR(); wf[n][1] = K1_RA(1, kn); SBAR();
                    acc[2] = MFMA(wf[c][2], xf[c], acc[2]); SBAR(); wf[n][2] = K1_RB(0, kn); SBAR();
                    acc[3] = MFMA(wf[c][3], xf[c], acc[3]); SBAR(); wf[n][3] = K1_RB(1, kn); SBAR();
                } else {
                    acc[0] = MFMA(xf[c], wf[c][0], acc[0]); SBAR(); xf[n] = lds_b128(sb + xa[kn]); wf[n][0] = K1_RA(2, kn); SBAR();
                    acc[1] = MFMA(wf[c][1], xf[c], acc[1]); SBAR(); wf[n][1] = K1_RB(2, kn); SBAR();
                    if (third) acc[2] = MFMA(wf[c][2], xf[c], acc[2]);
                    SBAR(); wf[n][2] = dogate ? K1_RG(kt_r, kn) : K1_RB(3, kn); SBAR();
                }
            }
            buf = nb;
        }
    };
    if (wc == 0) run(std::integral_constant<int, 0>{}); else run(std::integral_constant<int, 1>{});
    const int tok0 = panel * 128 + wt * 32;
    if (wc == 0) {
#pragma unroll
        for (int s = 0; s < 2; ++s) {
            const float sc = s == 0 ? QSCALE : 1.f;
            char* base = ws + (size_t)s * (S_ * D_ * 2);
#pragma unroll
            for (int hf = 0; hf < 2; ++hf) {
                u32x4 pk;
#pragma unroll
                for (int e = 0; e < 4; ++e) pk[e] = pack2(acc[s][8 * hf + 2 * e] * sc, acc[s][8 * hf + 2 * e + 1] * sc);
                st16_wt(base + ((size_t)(2 * cg + hf) * S_ + tok0) * 32 + lane * 16, pk);
            }
        }
    } else {
        char* vb = ws + WS_V + ((size_t)(2 * cg + (r >> 4)) * S_ + tok0) * 32 + h * 256 + (r & 15) * 16;
#pragma unroll
        for (int st = 0; st < 2; ++st) {
            u32x4 pk;
#pragma unroll
            for (int e = 0; e < 4; ++e) pk[e] = pack2(acc[0][8 * st + 2 * e], acc[0][8 * st + 2 * e + 1]);
            st16_wt(vb + st * 512, pk);
        }
        if (dogate && h == 0) ((float*)(ws + WS_G))[tok0 + r] = 1.f / (1.f + __expf(-acc[2][0]));
    }
    {
        lds_ptr stg = (lds_ptr)smem_ + wid * (32 * 144);
        const int zfirst = wc == 0 ? 0 : 2, zn = wc == 0 ? 2 : nz - 2;
        auto stage = [&](const f32x16& A, int s) {
#pragma unroll
            for (int g = 0; g < 4; ++g) {
                u32x2 pk;
                pk[0] = pack2(gelu_tanh(A[4 * g]), gelu_tanh(A[4 * g + 1]));
                pk[1] = pack2(gelu_tanh(A[4 * g + 2]), gelu_tanh(A[4 * g + 3]));
                *(LDSAS u32x2*)(stg + r * 144 + s * 64 + g * 16 + h * 8) = pk;
            }
        };
        if (wc == 0) { stage(acc[2], 0); stage(acc[3], 1); } else { stage(acc[1], 0); if (nz == 4) stage(acc[2], 1); }
        const bool second = zs >= 28;
        char* zb = ws + (second ? WS_Z2 : WS_Z1) + (size_t)tok0 * (FH * 2) + (zs + zfirst - (second ? 28 : 0)) * 64;
        const int cc = lane & 7;
        if (cc < zn * 4) {
#pragma unroll
            for (int i = 0; i < 4; ++i) {
                const int row = i * 8 + (lane >> 3);
                const u32x4 v = *(const LDSAS u32x4*)(stg + row * 144 + cc * 16);
                st16_wt(zb + (size_t)row * (FH * 2) + cc * 16, v);
            }
        }
    }
}

constexpr int AT_NS = 7;
constexpr int AT_RING = AT_NS * 8192;
constexpr int AT_FLAGS = AT_RING;
constexpr int AT_LDS = AT_RING + 64;
#define PIN(x) asm volatile("" : "+v"(x))
#define EX2(x) __builtin_amdgcn_exp2f(x)
#define BC8(x) __builtin_bit_cast(bf16x8, x)

DI float half_max(float m) {
    auto rr = __builtin_amdgcn_permlane32_swap(__float_as_uint(m), __float_as_uint(m), false, false);
    return __builtin_fmaxf(__uint_as_float(rr[0]), __uint_as_float(rr[1]));
}
DI float max16(const f32x16& s) {
    float a = __builtin_fmaxf(s[0], s[1]);
#pragma unroll
    for (int i = 2; i < 16; ++i) a = __builtin_fmaxf(a, s[i]);
    return a;
}
DI void exp_pack(const f32x16& s, bf16x8& p0, bf16x8& p1) {
    u32x4 a, b;
#pragma unroll
    for (int e = 0; e < 4; ++e) a[e] = pack2(EX2(s[2 * e]), EX2(s[2 * e + 1]));
#pragma unroll
    for (int e = 0; e < 4; ++e) b[e] = pack2(EX2(s[8 + 2 * e]), EX2(s[8 + 2 * e + 1]));
    p0 = __builtin_bit_cast(bf16x8, a); p1 = __builtin_bit_cast(bf16x8, b);
}

DI void attn_safe(const char* Kh, const char* Vh, bf16x8 qf, int lane, f32x16& Oout) {
    const int r = lane & 31, h = lane >> 5;
    f32x16 O = f32x16{}; float m = -INFINITY;
    const f32x16 zero = f32x16{};
    const u32x4 ones = u32x4{0x3f803f80u, 0x3f803f80u, 0x3f803f80u, 0x3f803f80u};
    for (int t = 0; t < S_ / 32; ++t) {
        const bf16x8 kf = *(const bf16x8*)(Kh + t * 1024 + lane * 16);
        f32x16 s = MFMA(kf, qf, zero);
        const float mn = __builtin_fmaxf(m, half_max(max16(s)));
        const float al = EX2(m - mn);
        m = mn;
#pragma unroll
        for (int i = 0; i < 16; ++i) { O[i] *= al; s[i] -= mn; }
        bf16x8 p0, p1; exp_pack(s, p0, p1);
        const u32x4 a0 = *(const u32x4*)(Vh + t * 1024 + h * 256 + (r & 15) * 16), a1 = *(const u32x4*)(Vh + t * 1024 + 512 + h * 256 + (r & 15) * 16);
        O = MFMA(BC8(r < 16 ? a0 : ones), p0, O); O = MFMA(BC8(r < 16 ? a1 : ones), p1, O);
    }
    Oout = O;
}

#define ATTN_LOOP_ASM \
    "s_mov_b32 s40, 8\n" \
    "s_mov_b32 s41, 6\n" \
    "s_mov_b32 s53, 0\n" \
    "s_mov_b32 s54, 49152\n" \
    "v_mov_b32 v16, v20\n" \
    "v_mov_b32 v18, v21\n" \
    "s_nop 15\n" \
    "1:\n" \
    "s_add_u32 s53, s53, 8192\n" \
    "s_cmp_eq_u32 s53, 57344\n" \
    "s_cselect_b32 s53, 0, s53\n" \
    "s_waitcnt lgkmcnt(2)\n" \
    "v_mfma_f32_32x32x16_bf16 v[80:95], v[112:115], v[108:111], v[48:63]\n" \
    "ds_read_b128 v[112:115], v16 offset:2048\n" \
    "v_exp_f32 v64, v64\n" \
    "v_exp_f32 v65, v65\n" \
    "v_exp_f32 v66, v66\n" \
    "v_exp_f32 v67, v67\n" \
    "v_cvt_pk_bf16_f32 v102, v104, v105\n" \
    "v_cvt_pk_bf16_f32 v103, v106, v107\n" \
    "v_exp_f32 v68, v68\n" \
    "v_exp_f32 v69, v69\n" \
    "s_waitcnt lgkmcnt(2)\n" \
    "v_mfma_f32_32x32x16_bf16 v[32:47], v[124:127], v[100:103], v[32:47]\n" \
    "s_mov_b64 exec, s[48:49]\n" \
    "ds_read_b128 v[124:127], v18 offset:4608\n" \
    "ds_read_b128 v[120:123], v18 offset:5120\n" \
    "s_mov_b64 exec, -1\n" \
    "s_waitcnt vmcnt(4)\n" \
    "ds_write_b64 v28, v[26:27]\n" \
    "v_exp_f32 v70, v70\n" \
    "v_exp_f32 v71, v71\n" \
    "v_cvt_pk_bf16_f32 v96, v64, v65\n" \
    "v_cvt_pk_bf16_f32 v97, v66, v67\n" \
    "v_exp_f32 v72, v72\n" \
    "v_exp_f32 v73, v73\n" \
    "v_cvt_pk_bf16_f32 v98, v68, v69\n" \
    "v_cvt_pk_bf16_f32 v99, v70, v71\n" \
    "v_exp_f32 v74, v74\n" \
    "v_exp_f32 v75, v75\n" \
    "s_waitcnt lgkmcnt(3)\n" \
    "v_mfma_f32_32x32x16_bf16 v[32:47], v[116:119], v[96:99], v[32:47]\n" \
    "v_add_u32 v26, 1, v26\n" \
    "v_add_u32 v27, 1, v27\n" \
    "v_add_u32 v17, s53, v20\n" \
    "v_add_u32 v19, s53, v21\n" \
    "v_exp_f32 v104, v76\n" \
    "v_exp_f32 v105, v77\n" \
    "v_cvt_pk_bf16_f32 v100, v72, v73\n" \
    "v_cvt_pk_bf16_f32 v101, v74, v75\n" \
    "v_exp_f32 v106, v78\n" \
    "v_exp_f32 v107, v79\n" \
    "s_waitcnt lgkmcnt(2)\n" \
    "v_mfma_f32_32x32x16_bf16 v[64:79], v[112:115], v[108:111], v[48:63]\n" \
    "ds_read_b128 v[112:115], v16 offset:3072\n" \
    "ds_read_b32 v23, v22\n" \
    "v_exp_f32 v80, v80\n" \
    "v_exp_f32 v81, v81\n" \
    "v_exp_f32 v82, v82\n" \
    "v_exp_f32 v83, v83\n" \
    "v_cvt_pk_bf16_f32 v102, v104, v105\n" \
    "v_cvt_pk_bf16_f32 v103, v106, v107\n" \
    "v_exp_f32 v84, v84\n" \
    "v_exp_f32 v85, v85\n" \
    "s_waitcnt lgkmcnt(2)\n" \
    "v_mfma_f32_32x32x16_bf16 v[32:47], v[124:127], v[100:103], v[32:47]\n" \
    "s_mov_b64 exec, s[48:49]\n" \
    "ds_read_b128 v[124:127], v18 offset:5632\n" \
    "ds_read_b128 v[116:119], v18 offset:6144\n" \
    "s_mov_b64 exec, -1\n" \
    "v_exp_f32 v86, v86\n" \
    "v_exp_f32 v87, v87\n" \
    "v_cvt_pk_bf16_f32 v96, v80, v81\n" \
    "v_cvt_pk_bf16_f32 v97, v82, v83\n" \
    "v_exp_f32 v88, v88\n" \
    "v_exp_f32 v89, v89\n" \
    "v_cvt_pk_bf16_f32 v98, v84, v85\n" \
    "v_cvt_pk_bf16_f32 v99, v86, v87\n" \
    "v_exp_f32 v90, v90\n" \
    "v_exp_f32 v91, v91\n" \
    "s_waitcnt lgkmcnt(3)\n" \
    "v_mfma_f32_32x32x16_bf16 v[32:47], v[120:123], v[96:99], v[32:47]\n" \
    "v_exp_f32 v104, v92\n" \
    "v_exp_f32 v105, v93\n" \
    "v_cvt_pk_bf16_f32 v100, v88, v89\n" \
    "v_cvt_pk_bf16_f32 v101, v90, v91\n" \
    "v_exp_f32 v106, v94\n" \
    "v_exp_f32 v107, v95\n" \
    "s_waitcnt lgkmcnt(2)\n" \
    "v_mfma_f32_32x32x16_bf16 v[80:95], v[112:115], v[108:111], v[48:63]\n" \
    "v_cmp_lt_u32 vcc, v23, v24\n" \
    "s_cbranch_vccnz 20f\n" \
    "30:\n" \
    "ds_read_b128 v[112:115], v17 offset:0\n" \
    "s_and_b32 s47, s41, 15\n" \
    "s_lshl_b32 s47, s47, 12\n" \
    "s_add_u32 s44, s42, s47\n" \
    "s_addc_u32 s45, s43, 0\n" \
    "s_add_u32 m0, s50, s54\n" \
    "s_add_u32 s41, s41, 1\n" \
    "global_load_lds_dwordx4 v29, s[44:45]\n" \
    "s_add_u32 s54, s54, 8192\n" \
    "s_cmp_eq_u32 s54, 57344\n" \
    "s_cselect_b32 s54, 0, s54\n" \
    "v_exp_f32 v64, v64\n" \
    "v_exp_f32 v65, v65\n" \
    "v_exp_f32 v66, v66\n" \
    "v_exp_f32 v67, v67\n" \
    "v_cvt_pk_bf16_f32 v102, v104, v105\n" \
    "v_cvt_pk_bf16_f32 v103, v106, v107\n" \
    "v_exp_f32 v68, v68\n" \
    "v_exp_f32 v69, v69\n" \
    "s_waitcnt lgkmcnt(2)\n" \
    "v_mfma_f32_32x32x16_bf16 v[32:47], v[124:127], v[100:103], v[32:47]\n" \
    "s_mov_b64 exec, s[48:49]\n" \
    "ds_read_b128 v[124:127], v18 offset:6656\n" \
    "ds_read_b128 v[120:123], v18 offset:7168\n" \
    "s_mov_b64 exec, -1\n" \
    "v_exp_f32 v70, v70\n" \
    "v_exp_f32 v71, v71\n" \
    "v_cvt_pk_bf16_f32 v96, v64, v65\n" \
    "v_cvt_pk_bf16_f32 v97, v66, v67\n" \
    "v_exp_f32 v72, v72\n" \
    "v_exp_f32 v73, v73\n" \
    "v_cvt_pk_bf16_f32 v98, v68, v69\n" \
    "v_cvt_pk_bf16_f32 v99, v70, v71\n" \
    "v_exp_f32 v74, v74\n" \
    "v_exp_f32 v75, v75\n" \
    "s_waitcnt lgkmcnt(3)\n" \
    "v_mfma_f32_32x32x16_bf16 v[32:47], v[116:119], v[96:99], v[32:47]\n" \
    "v_add_u32 v24, 1, v24\n" \
    "v_exp_f32 v104, v76\n" \
    "v_exp_f32 v105, v77\n" \
    "v_cvt_pk_bf16_f32 v100, v72, v73\n" \
    "v_cvt_pk_bf16_f32 v101, v74, v75\n" \
    "v_exp_f32 v106, v78\n" \
    "v_exp_f32 v107, v79\n" \
    "s_waitcnt lgkmcnt(2)\n" \
    "v_mfma_f32_32x32x16_bf16 v[64:79], v[112:115], v[108:111], v[48:63]\n" \
    "ds_read_b128 v[112:115], v17 offset:1024\n" \
    "v_exp_f32 v80, v80\n" \
    "v_exp_f32 v81, v81\n" \
    "v_exp_f32 v82, v82\n" \
    "v_exp_f32 v83, v83\n" \
    "v_cvt_pk_bf16_f32 v102, v104, v105\n" \
    "v_cvt_pk_bf16_f32 v103, v106, v107\n" \
    "v_exp_f32 v84, v84\n" \
    "v_exp_f32 v85, v85\n" \
    "s_waitcnt lgkmcnt(2)\n" \
    "v_mfma_f32_32x32x16_bf16 v[32:47], v[124:127], v[100:103], v[32:47]\n" \
    "s_mov_b64 exec, s[48:49]\n" \
    "ds_read_b128 v[124:127], v18 offset:7680\n" \
    "ds_read_b128 v[116:119], v19 offset:4096\n" \
    "s_mov_b64 exec, -1\n" \
    "v_exp_f32 v86, v86\n" \
    "v_exp_f32 v87, v87\n" \
    "v_cvt_pk_bf16_f32 v96, v80, v81\n" \
    "v_cvt_pk_bf16_f32 v97, v82, v83\n" \
    "v_exp_f32 v88, v88\n" \
    "v_exp_f32 v89, v89\n" \
    "v_cvt_pk_bf16_f32 v98, v84, v85\n" \
    "v_cvt_pk_bf16_f32 v99, v86, v87\n" \
    "v_exp_f32 v90, v90\n" \
    "v_exp_f32 v91, v91\n" \
    "s_waitcnt lgkmcnt(3)\n" \
    "v_mfma_f32_32x32x16_bf16 v[32:47], v[120:123], v[96:99], v[32:47]\n" \
    "v_exp_f32 v104, v92\n" \
    "v_exp_f32 v105, v93\n" \
    "v_cvt_pk_bf16_f32 v100, v88, v89\n" \
    "v_cvt_pk_bf16_f32 v101, v90, v91\n" \
    "v_exp_f32 v106, v94\n" \
    "v_exp_f32 v107, v95\n" \
    "s_add_u32 s53, s53, 8192\n" \
    "s_cmp_eq_u32 s53, 57344\n" \
    "s_cselect_b32 s53, 0, s53\n" \
    "s_waitcnt lgkmcnt(2)\n" \
    "v_mfma_f32_32x32x16_bf16 v[80:95], v[112:115], v[108:111], v[48:63]\n" \
    "ds_read_b128 v[112:115], v17 offset:2048\n" \
    "v_exp_f32 v64, v64\n" \
    "v_exp_f32 v65, v65\n" \
    "v_exp_f32 v66, v66\n" \
    "v_exp_f32 v67, v67\n" \
    "v_cvt_pk_bf16_f32 v102, v104, v105\n" \
    "v_cvt_pk_bf16_f32 v103, v106, v107\n" \
    "v_exp_f32 v68, v68\n" \
    "v_exp_f32 v69, v69\n" \
    "s_waitcnt lgkmcnt(2)\n" \
    "v_mfma_f32_32x32x16_bf16 v[32:47], v[124:127], v[100:103], v[32:47]\n" \
    "s_mov_b64 exec, s[48:49]\n" \
    "ds_read_b128 v[124:127], v19 offset:4608\n" \
    "ds_read_b128 v[120:123], v19 offset:5120\n" \
    "s_mov_b64 exec, -1\n" \
    "s_waitcnt vmcnt(4)\n" \
    "ds_write_b64 v28, v[26:27]\n" \
    "v_exp_f32 v70, v70\n" \
    "v_exp_f32 v71, v71\n" \
    "v_cvt_pk_bf16_f32 v96, v64, v65\n" \
    "v_cvt_pk_bf16_f32 v97, v66, v67\n" \
    "v_exp_f32 v72, v72\n" \
    "v_exp_f32 v73, v73\n" \
    "v_cvt_pk_bf16_f32 v98, v68, v69\n" \
    "v_cvt_pk_bf16_f32 v99, v70, v71\n" \
    "v_exp_f32 v74, v74\n" \
    "v_exp_f32 v75, v75\n" \
    "s_waitcnt lgkmcnt(3)\n" \
    "v_mfma_f32_32x32x16_bf16 v[32:47], v[116:119], v[96:99], v[32:47]\n" \
    "v_add_u32 v26, 1, v26\n" \
    "v_add_u32 v27, 1, v27\n" \
    "v_add_u32 v16, s53, v20\n" \
    "v_add_u32 v18, s53, v21\n" \
    "v_exp_f32 v104, v76\n" \
    "v_exp_f32 v105, v77\n" \
    "v_cvt_pk_bf16_f32 v100, v72, v73\n" \
    "v_cvt_pk_bf16_f32 v101, v74, v75\n" \
    "v_exp_f32 v106, v78\n" \
    "v_exp_f32 v107, v79\n" \
    "s_waitcnt lgkmcnt(2)\n" \
    "v_mfma_f32_32x32x16_bf16 v[64:79], v[112:115], v[108:111], v[48:63]\n" \
    "ds_read_b128 v[112:115], v17 offset:3072\n" \
    "ds_read_b32 v23, v22\n" \
    "v_exp_f32 v80, v80\n" \
    "v_exp_f32 v81, v81\n" \
    "v_exp_f32 v82, v82\n" \
    "v_exp_f32 v83, v83\n" \
    "v_cvt_pk_bf16_f32 v102, v104, v105\n" \
    "v_cvt_pk_bf16_f32 v103, v106, v107\n" \
    "v_exp_f32 v84, v84\n" \
    "v_exp_f32 v85, v85\n" \
    "s_waitcnt lgkmcnt(2)\n" \
    "v_mfma_f32_32x32x16_bf16 v[32:47], v[124:127], v[100:103], v[32:47]\n" \
    "s_mov_b64 exec, s[48:49]\n" \
    "ds_read_b128 v[124:127], v19 offset:5632\n" \
    "ds_read_b128 v[116:119], v19 offset:6144\n" \
    "s_mov_b64 exec, -1\n" \
    "v_exp_f32 v86, v86\n" \
    "v_exp_f32 v87, v87\n" \
    "v_cvt_pk_bf16_f32 v96, v80, v81\n" \
    "v_cvt_pk_bf16_f32 v97, v82, v83\n" \
    "v_exp_f32 v88, v88\n" \
    "v_exp_f32 v89, v89\n" \
    "v_cvt_pk_bf16_f32 v98, v84, v85\n" \
    "v_cvt_pk_bf16_f32 v99, v86, v87\n" \
    "v_exp_f32 v90, v90\n" \
    "v_exp_f32 v91, v91\n" \
    "s_waitcnt lgkmcnt(3)\n" \
    "v_mfma_f32_32x32x16_bf16 v[32:47], v[120:123], v[96:99], v[32:47]\n" \
    "v_exp_f32 v104, v92\n" \
    "v_exp_f32 v105, v93\n" \
    "v_cvt_pk_bf16_f32 v100, v88, v89\n" \
    "v_cvt_pk_bf16_f32 v101, v90, v91\n" \
    "v_exp_f32 v106, v94\n" \
    "v_exp_f32 v107, v95\n" \
    "s_waitcnt lgkmcnt(2)\n" \
    "v_mfma_f32_32x32x16_bf16 v[80:95], v[112:115], v[108:111], v[48:63]\n" \
    "v_cmp_lt_u32 vcc, v23, v24\n" \
    "s_cbranch_vccnz 21f\n" \
    "31:\n" \
    "ds_read_b128 v[112:115], v16 offset:0\n" \
    "s_and_b32 s47, s41, 15\n" \
    "s_lshl_b32 s47, s47, 12\n" \
    "s_add_u32 s44, s42, s47\n" \
    "s_addc_u32 s45, s43, 0\n" \
    "s_add_u32 m0, s50, s54\n" \
    "s_add_u32 s41, s41, 1\n" \
    "global_load_lds_dwordx4 v29, s[44:45]\n" \
    "s_add_u32 s54, s54, 8192\n" \
    "s_cmp_eq_u32 s54, 57344\n" \
    "s_cselect_b32 s54, 0, s54\n" \
    "v_exp_f32 v64, v64\n" \
    "v_exp_f32 v65, v65\n" \
    "v_exp_f32 v66, v66\n" \
    "v_exp_f32 v67, v67\n" \
    "v_cvt_pk_bf16_f32 v102, v104, v105\n" \
    "v_cvt_pk_bf16_f32 v103, v106, v107\n" \
    "v_exp_f32 v68, v68\n" \
    "v_exp_f32 v69, v69\n" \
    "s_waitcnt lgkmcnt(2)\n" \
    "v_mfma_f32_32x32x16_bf16 v[32:47], v[124:127], v[100:103], v[32:47]\n" \
    "s_mov_b64 exec, s[48:49]\n" \
    "ds_read_b128 v[124:127], v19 offset:6656\n" \
    "ds_read_b128 v[120:123], v19 offset:7168\n" \
    "s_mov_b64 exec, -1\n" \
    "v_exp_f32 v70, v70\n" \
    "v_exp_f32 v71, v71\n" \
    "v_cvt_pk_bf16_f32 v96, v64, v65\n" \
    "v_cvt_pk_bf16_f32 v97, v66, v67\n" \
    "v_exp_f32 v72, v72\n" \
    "v_exp_f32 v73, v73\n" \
    "v_cvt_pk_bf16_f32 v98, v68, v69\n" \
    "v_cvt_pk_bf16_f32 v99, v70, v71\n" \
    "v_exp_f32 v74, v74\n" \
    "v_exp_f32 v75, v75\n" \
    "s_waitcnt lgkmcnt(3)\n" \
    "v_mfma_f32_32x32x16_bf16 v[32:47], v[116:119], v[96:99], v[32:47]\n" \
    "v_add_u32 v24, 1, v24\n" \
    "v_exp_f32 v104, v76\n" \
    "v_exp_f32 v105, v77\n" \
    "v_cvt_pk_bf16_f32 v100, v72, v73\n" \
    "v_cvt_pk_bf16_f32 v101, v74, v75\n" \
    "v_exp_f32 v106, v78\n" \
    "v_exp_f32 v107, v79\n" \
    "s_waitcnt lgkmcnt(2)\n" \
    "v_mfma_f32_32x32x16_bf16 v[64:79], v[112:115], v[108:111], v[48:63]\n" \
    "ds_read_b128 v[112:115], v16 offset:1024\n" \
    "v_exp_f32 v80, v80\n" \
    "v_exp_f32 v81, v81\n" \
    "v_exp_f32 v82, v82\n" \
    "v_exp_f32 v83, v83\n" \
    "v_cvt_pk_bf16_f32 v102, v104, v105\n" \
    "v_cvt_pk_bf16_f32 v103, v106, v107\n" \
    "v_exp_f32 v84, v84\n" \
    "v_exp_f32 v85, v85\n" \
    "s_waitcnt lgkmcnt(2)\n" \
    "v_mfma_f32_32x32x16_bf16 v[32:47], v[124:127], v[100:103], v[32:47]\n" \
    "s_mov_b64 exec, s[48:49]\n" \
    "ds_read_b128 v[124:127], v19 offset:7680\n" \
    "ds_read_b128 v[116:119], v18 offset:4096\n" \
    "s_mov_b64 exec, -1\n" \
    "v_exp_f32 v86, v86\n" \
    "v_exp_f32 v87, v87\n" \
    "v_cvt_pk_bf16_f32 v96, v80, v81\n" \
    "v_cvt_pk_bf16_f32 v97, v82, v83\n" \
    "v_exp_f32 v88, v88\n" \
    "v_exp_f32 v89, v89\n" \
    "v_cvt_pk_bf16_f32 v98, v84, v85\n" \
    "v_cvt_pk_bf16_f32 v99, v86, v87\n" \
    "v_exp_f32 v90, v90\n" \
    "v_exp_f32 v91, v91\n" \
    "s_waitcnt lgkmcnt(3)\n" \
    "v_mfma_f32_32x32x16_bf16 v[32:47], v[120:123], v[96:99], v[32:47]\n" \
    "v_exp_f32 v104, v92\n" \
    "v_exp_f32 v105, v93\n" \
    "v_cvt_pk_bf16_f32 v100, v88, v89\n" \
    "v_cvt_pk_bf16_f32 v101, v90, v91\n" \
    "v_exp_f32 v106, v94\n" \
    "v_exp_f32 v107, v95\n" \
    "s_sub_u32 s40, s40, 1\n" \
    "s_cmp_lg_u32 s40, 0\n" \
    "s_cbranch_scc1 1b\n" \
    "v_cvt_pk_bf16_f32 v102, v104, v105\n" \
    "v_cvt_pk_bf16_f32 v103, v106, v107\n" \
    "s_waitcnt lgkmcnt(0)\n" \
    "v_mfma_f32_32x32x16_bf16 v[32:47], v[124:127], v[100:103], v[32:47]\n" \
    "s_waitcnt vmcnt(0)\n" \
    "s_branch 9f\n" \
    "20:\n" \
    "s_mov_b32 s51, 0x400000\n" \
    "40:\n" \
    "s_sleep 1\n" \
    "ds_read_b32 v23, v22\n" \
    "s_waitcnt lgkmcnt(0)\n" \
    "v_cmp_lt_u32 vcc, v23, v24\n" \
    "s_cbranch_vccz 30b\n" \
    "s_sub_u32 s51, s51, 1\n" \
    "s_cmp_lg_u32 s51, 0\n" \
    "s_cbranch_scc1 40b\n" \
    "s_branch 30b\n" \
    "21:\n" \
    "s_mov_b32 s51, 0x400000\n" \
    "41:\n" \
    "s_sleep 1\n" \
    "ds_read_b32 v23, v22\n" \
    "s_waitcnt lgkmcnt(0)\n" \
    "v_cmp_lt_u32 vcc, v23, v24\n" \
    "s_cbranch_vccz 31b\n" \
    "s_sub_u32 s51, s51, 1\n" \
    "s_cmp_lg_u32 s51, 0\n" \
    "s_cbranch_scc1 41b\n" \
    "s_branch 31b\n" \
    "9:\n" \
    "s_nop 15\n"

#define B_PRIO 2
#define B_HI() __builtin_amdgcn_s_setprio(B_PRIO)
#define B_LO() __builtin_amdgcn_s_setprio(0)
#define BMFMA MFMA
#define B_GO(x) (x)
#define glds16_z glds16_sc1
constexpr int B_FLAGS = AT_RING + 64;
constexpr int B_RING = AT_RING + 128;
constexpr int SP_AB = 128 * 128, SP_ZB = 64 * 128, SP_STAGE = SP_AB + SP_ZB, SP_NST = 4;
constexpr int K2_LDS = B_RING + SP_NST * SP_STAGE;
constexpr int NRM_STG = B_RING + 3 * SP_STAGE;

DI void attn_role(const char* __restrict__ ws_in, char* __restrict__ ws, lds_cptr smem, unsigned lds0, int wid, int lane) {
    const int b = blockIdx.x, head = (b & 7) * 4 + ((b >> 3) & 3), qr = b >> 5;
    const int r = lane & 31, h = lane >> 5;
    const char* Kh = ws_in + WS_K + (size_t)head * S_ * 32;
    const char* Vh = ws_in + WS_V + (size_t)head * S_ * 32;
    const char* Qh = ws_in + WS_Q + ((size_t)head * S_ + qr * 256 + wid * 32) * 32;
    const char* src0 = (wid < 4 ? Kh : Vh) + (wid & 3) * 1024;
    const unsigned voff = lane * 16;
    const unsigned m0b = lds0 + wid * 1024;
    u32x4 qraw;
    asm volatile("global_load_dwordx4 %0, %1, %2" : "=&v"(qraw) : "v"(voff), "s"(Qh) : "memory");
#pragma unroll
    for (int c = 0; c < AT_NS - 1; ++c) glds16(src0 + c * 4096, voff, m0b + c * 8192);
    const unsigned ka = lds0 + lane * 16, va = lds0 + h * 256 + (r & 15) * 16;
    const unsigned o1 = r == 16 ? 0x3f803f80u : 0u;
    const u32x4 ones = u32x4{o1, o1, o1, o1};
    const unsigned fa = lds0 + AT_FLAGS + (lane & 15) * 4, puba = lds0 + AT_FLAGS + wid * 8;

    f32x16 O, C, SA, SB_;
    u32x4 p0, p1 = u32x4{0, 0, 0, 0}, T = u32x4{0, 0, 0, 0}, kf, v0a, v0b = ones, v1 = ones;
    asm volatile("s_waitcnt vmcnt(%1)" : "+v"(qraw) : "n"(AT_NS - 2) : "memory");
    const bf16x8 qf = BC8(qraw);
    *(volatile LDSAS unsigned*)(size_t)puba = 1u;
    {
        unsigned spins = 0;
        while (true) {
            CFENCE();
            const unsigned f = *(volatile LDSAS unsigned*)(size_t)fa;
            if (!__any((lane & 1) == 0 && f < 1u) || ++spins > (1u << 22)) break;
            __builtin_amdgcn_s_sleep(1);
        }
        CFENCE();
    }
    {
        kf = *(const LDSAS u32x4*)(smem + lane * 16);
        const f32x16 sa = MFMA(BC8(kf), qf, f32x16{});
        const float ma = half_max(max16(sa));
#pragma unroll
        for (int i = 0; i < 16; ++i) { C[i] = -ma; O[i] = 0.f; }
        SA = MFMA(BC8(kf), qf, C);
        kf = *(const LDSAS u32x4*)(smem + lane * 16 + 1024);
        v0a = *(const LDSAS u32x4*)(smem + 4096 + h * 256 + (r & 15) * 16);
        if (r >= 16) v0a = ones;
    }
    {
        unsigned thr = (lane & 1) ? 0u : 2u, fv = 0;
        u32x2 pub = u32x2{2u, 0u};
        asm volatile(ATTN_LOOP_ASM
            : "+{v[32:47]}"(O), "+{v[64:79]}"(SA), "={v[80:95]}"(SB_), "={v[96:99]}"(p0), "+{v[100:103]}"(p1), "+{v[104:107]}"(T), "+{v[112:115]}"(kf),
              "+{v[116:119]}"(v0a), "+{v[120:123]}"(v0b), "+{v[124:127]}"(v1), "+{v23}"(fv), "+{v24}"(thr), "+{v[26:27]}"(pub)
            : "{v[48:63]}"(C), "{v[108:111]}"(qf), "{v20}"(ka), "{v21}"(va), "{v22}"(fa), "{v28}"(puba), "{v29}"(voff),
              "{s[42:43]}"(src0), "{s50}"(m0b), "{s[48:49]}"(0x0000ffff0000ffffull)
            : "v16", "v17", "v18", "v19", "s40", "s41", "s44", "s45", "s47", "s51", "s53", "s54", "vcc", "scc", "memory");
    }
    {
        auto rr = __builtin_amdgcn_permlane32_swap(__float_as_uint(O[8]), __float_as_uint(O[8]), false, false);
        float l = __uint_as_float(rr[0]) + __uint_as_float(rr[1]);
        bool bad = !(l < 1e30f) || !(l > 1e-30f);
#pragma unroll
        for (int i = 0; i < 8; ++i) bad |= !(__builtin_fabsf(O[i]) < 3e38f);
        if (__any(bad)) { attn_safe(Kh, Vh, qf, lane, O); l = O[8]; }
        const float inv = 1.f / l;
        u32x4 pk;
#pragma unroll
        for (int i = 0; i < 4; ++i) pk[i] = pack2(O[2 * i] * inv, O[2 * i + 1] * inv);
        const int t = qr * 256 + wid * 32 + r;
        st16_wt(ws + WS_O + (size_t)t * 1024 + head * 32 + h * 16, pk);
    }
    wait_vm<0>();
    {
        unsigned old = 0;
        if (lane == 0) old = __hip_atomic_fetch_add((LDSAS unsigned*)(size_t)(lds0 + B_FLAGS + 28), 1u, __ATOMIC_RELAXED, __HIP_MEMORY_SCOPE_WORKGROUP);
        old = __builtin_amdgcn_readfirstlane(old);
        if (old == 7 && lane == 0) __hip_atomic_fetch_add((gu32*)(ws + WS_FLAG) + (FL_CO + qr) * 32, 1u, RLX_AGENT);
    }
}

DI void b_role(const u16* __restrict__ wsp, const u16* __restrict__ bsp, char* __restrict__ ws,
               lds_cptr smem, unsigned lds0, int bw, int lane) {
    const int b = blockIdx.x, xi = b & 7, j = b >> 3;
    const bool has_tile = j < 28;
    const int ti = j & 3, fi = j >> 2;
    const int tt = 4 * (xi & 3) + ti, ft = 7 * (xi >> 2) + fi, rot = 4 * fi + ti;
    const int r = lane & 31, h = lane >> 5, wt = bw & 1, wf = bw >> 1;
    const char* abase = (const char*)(wsp + (size_t)tt * 128 * S_);
    const char* zbase = ws + WS_Z2N + ft * 1024;
    unsigned aoff[4], zoff[2];
#pragma unroll
    for (int i = 0; i < 4; ++i) { const int row = (bw * 4 + i) * 8 + (lane >> 3), c = lane & 7; aoff[i] = row * (S_ * 2) + ((c ^ ((row >> 1) & 7)) << 4); }
#pragma unroll
    for (int i = 0; i < 2; ++i) zoff[i] = (bw * 2 + i) * (FH * 16) + lane * 16;
    const unsigned ring = lds0 + B_RING;
    auto issueA = [&](int s, int slot) {
        const char* ab = abase + ((s + rot) & 31) * 128;
        const unsigned la = ring + slot * SP_STAGE + bw * 4096;
#pragma unroll
        for (int i = 0; i < 4; ++i) glds16(ab, aoff[i], la + i * 1024);
    };
    auto issueZ = [&](int s, int slot) {
        const char* zb = zbase + (size_t)((s + rot) & 31) * (8 * FH * 16);
        const unsigned lz = ring + slot * SP_STAGE + SP_AB + bw * 2048;
#pragma unroll
        for (int i = 0; i < 2; ++i) glds16_z(zb, zoff[i], lz + i * 1024);
    };
    {
        const bool act = lane < 56;
        const int ln = act ? lane : 55;
        const char* src = ws + WS_Z2 + (size_t)(8 * b + 2 * bw) * (FH * 2) + ln * 16;
        u32x4 v[2][2];
#pragma unroll
        for (int rr = 0; rr < 2; ++rr) { v[rr][0] = *(const u32x4*)(src + rr * (FH * 2)); v[rr][1] = *(const u32x4*)(src + rr * (FH * 2) + 896); }
        float xs[2][16], sm[2], sq[2];
#pragma unroll
        for (int rr = 0; rr < 2; ++rr) {
            const float piv = __uint_as_float(__builtin_amdgcn_readfirstlane(v[rr][0][0] << 16));
            float s = 0.f, q = 0.f;
#pragma unroll
            for (int i = 0; i < 2; ++i)
#pragma unroll
                for (int e = 0; e < 4; ++e) {
                    const float a = bf_lo(v[rr][i][e]) - piv, c = bf_hi(v[rr][i][e]) - piv;
                    xs[rr][8 * i + 2 * e] = a; xs[rr][8 * i + 2 * e + 1] = c;
                    s += a + c; q = __builtin_fmaf(a, a, q); q = __builtin_fmaf(c, c, q);
                }
            sm[rr] = act ? s : 0.f; sq[rr] = act ? q : 0.f;
        }
        sm[0] = wave_sum(sm[0]); sm[1] = wave_sum(sm[1]); sq[0] = wave_sum(sq[0]); sq[1] = wave_sum(sq[1]);
#pragma unroll
        for (int rr = 0; rr < 2; ++rr) {
            const float mean = sm[rr] * (1.f / FH);
            const float var = __builtin_fmaxf(__builtin_fmaf(-mean, mean, sq[rr] * (1.f / FH)), 0.f);
            const float rstd = __builtin_amdgcn_rsqf(var + 1e-5f), nm = -mean * rstd;
            if (act) {
#pragma unroll
                for (int i = 0; i < 2; ++i) {
                    u32x4 o4;
#pragma unroll
                    for (int e = 0; e < 4; ++e) o4[e] = pack2(__builtin_fmaf(xs[rr][8 * i + 2 * e], rstd, nm), __builtin_fmaf(xs[rr][8 * i + 2 * e + 1], rstd, nm));
                    *(LDSAS u32x4*)((lds_ptr)smem + NRM_STG + (2 * bw + rr) * (FH * 2) + i * 896 + lane * 16) = o4;
                }
            }
        }
    }
    LDSAS unsigned* bfl = (LDSAS unsigned*)(size_t)(lds0 + B_FLAGS);
    {
        asm volatile("s_waitcnt lgkmcnt(0)" ::: "memory");
        if (lane == 0) __hip_atomic_fetch_add(bfl + 6, 1u, __ATOMIC_RELAXED, __HIP_MEMORY_SCOPE_WORKGROUP);
        unsigned spins = 0;
        while (*(volatile LDSAS unsigned*)(bfl + 6) < 4u && ++spins < (1u << 24)) __builtin_amdgcn_s_sleep(1);
        CFENCE();
        const __amdgpu_buffer_rsrc_t zn = __builtin_amdgcn_make_buffer_rsrc(ws + WS_Z2N, 0, S_ * FH * 2, 0x00020000);
        const int i16 = lane & 15, gq = lane >> 4;
#pragma unroll
        for (int it = 0; it < 4; ++it) {
            const int bl = 4 * it + gq, blk = 14 * bw + (bl < 14 ? bl : 13);
            const bf16x8 col = lds_tr2(smem + NRM_STG + (i16 >> 2) * (FH * 2) + (blk * 16 + 4 * (i16 & 3)) * 2, 4 * FH * 2);
            if (bl < 14) __builtin_amdgcn_raw_buffer_store_b128(__builtin_bit_cast(u32x4, col), zn, b * (FH * 16) + (blk * 16 + i16) * 16, 0, 16);
        }
    }
    wait_vm<0>();
    {
        unsigned old = 0;
        if (lane == 0) old = __hip_atomic_fetch_add(bfl + 4, 1u, __ATOMIC_RELAXED, __HIP_MEMORY_SCOPE_WORKGROUP);
        old = __builtin_amdgcn_readfirstlane(old);
        if (old == 3 && lane < 8) __hip_atomic_fetch_add((gu32*)(ws + WS_FLAG) + (lane * 16 + ((b >> 3) & 15)) * 32, 1u, RLX_AGENT);
    }
    if (has_tile) {
    if (bw != 0) { issueA(0, 0); issueA(1, 1); }
    if (bw == 0) {
        gu32* fp = (gu32*)(ws + WS_FLAG) + (xi * 16 + (lane & 15)) * 32;
        unsigned spins = 0;
        unsigned f0 = __hip_atomic_load(fp, RLX_AGENT);
        while (true) {
            __builtin_amdgcn_s_sleep(2);
            const unsigned f1 = __hip_atomic_load(fp, RLX_AGENT);
            if (!__any(f0 != 16u) || ++spins > (1u << 20)) break;
            f0 = f1;
        }
        CFENCE();
        *(volatile LDSAS unsigned*)(bfl + 5) = 1u;
    } else {
        unsigned spins = 0;
        while (*(volatile LDSAS unsigned*)(bfl + 5) == 0u && ++spins < (1u << 24)) __builtin_amdgcn_s_sleep(4);
        CFENCE();
    }
    if (bw == 0) { issueA(0, 0); issueA(1, 1); }
    issueZ(0, 0); issueZ(1, 1);
    const int swz = (r >> 1) & 7;
    int aa[4];
#pragma unroll
    for (int ks = 0; ks < 4; ++ks) aa[ks] = B_RING + (wt * 64 + r) * 128 + (((2 * ks + h) ^ swz) << 4);
    const int za = B_RING + SP_AB + h * 1024 + (wf * 32 + r) * 16;
    f32x16 acc[2] = {f32x16{}, f32x16{}};
    volatile LDSAS unsigned* myfl = bfl + bw;
    volatile LDSAS unsigned* allfl = bfl + (lane & 3);
    wait_vm<2>();
    bf16x8 zf[2][4], a0[2][4], a1[2][4];
    auto check = [&](unsigned fl, unsigned need) {
        if (__builtin_expect(__any(fl < need), 0)) {
            unsigned spins = 0;
            do { __builtin_amdgcn_s_sleep(1); CFENCE(); fl = *allfl; } while (__any(fl < need) && ++spins < (1u << 20));
            CFENCE();
        }
    };
    *myfl = 1u;
    check(0u, 1u);
    lds_cptr pa[2][4], pz[2];
#pragma unroll
    for (int ks = 0; ks < 4; ++ks) { pa[0][ks] = smem + aa[ks]; pa[1][ks] = smem + aa[ks] + 2 * SP_STAGE; }
    pz[0] = smem + za; pz[1] = smem + za + 2 * SP_STAGE;
    unsigned fladdr = lds0 + B_FLAGS + bw * 4, one = 1u;
    asm volatile("" : "+v"(pa[0][0]), "+v"(pa[0][1]), "+v"(pa[0][2]), "+v"(pa[0][3]), "+v"(pa[1][0]), "+v"(pa[1][1]), "+v"(pa[1][2]), "+v"(pa[1][3]), "+v"(pz[0]), "+v"(pz[1]), "+v"(fladdr), "+v"(one));
#define B_PUB() asm volatile("s_mov_b64 exec, 1\n\tds_add_u32 %0, %1\n\ts_mov_b64 exec, -1" :: "v"(fladdr), "v"(one) : "memory")
#define B_GAP(ks, SL, cur, prv, FIRST, D0, D1) do { \
          if (!(FIRST)) { acc[0] = BMFMA(zf[prv][ks], a0[prv][ks], acc[0]); } SBAR(); \
          zf[cur][ks] = lds_b128(pz[(SL) >> 1] + ((SL) & 1) * SP_STAGE + (ks) * 2048); SBAR(); D0; SBAR(); \
          if (!(FIRST)) { acc[1] = BMFMA(zf[prv][ks], a1[prv][ks], acc[1]); } SBAR(); \
          a0[cur][ks] = lds_b128(pa[(SL) >> 1][ks] + ((SL) & 1) * SP_STAGE); a1[cur][ks] = lds_b128(pa[(SL) >> 1][ks] + ((SL) & 1) * SP_STAGE + 4096); SBAR(); D1; SBAR(); } while (0)
#define B_ITER(s, SL, cur, prv, FIRST, GO, VMW) do { \
        CFENCE(); B_LO(); SBAR(); \
        unsigned fl; \
        const char* ab = abase + (((s) + 2 + rot) & 31) * 128; const char* zb = zbase + (size_t)(((s) + 2 + rot) & 31) * (8 * FH * 16); \
        const unsigned la = ring + (((SL) + 2) & 3) * SP_STAGE + bw * 4096, lz = ring + (((SL) + 2) & 3) * SP_STAGE + SP_AB + bw * 2048; \
        B_GAP(0, SL, cur, prv, FIRST, if (B_GO(GO)) glds16(ab, aoff[0], la), if (B_GO(GO)) glds16(ab, aoff[1], la + 1024)); \
        B_GAP(1, SL, cur, prv, FIRST, if (B_GO(GO)) glds16(ab, aoff[2], la + 2048), if (B_GO(GO)) glds16(ab, aoff[3], la + 3072)); \
        asm volatile("s_waitcnt lgkmcnt(6)" ::: "memory");        \
        wait_vm<VMW>();                                            \
        B_PUB();                                                   \
        SBAR(); \
        B_GAP(2, SL, cur, prv, FIRST, if (B_GO(GO)) glds16_z(zb, zoff[0], lz), if (B_GO(GO)) glds16_z(zb, zoff[1], lz + 1024); fl = *allfl); \
        if (!B_GO(GO)) fl = *allfl; \
        B_GAP(3, SL, cur, prv, FIRST, (void)0, (void)0); \
        B_HI(); check(fl, (unsigned)((s) + 2)); SBAR(); \
    } while (0)
    B_ITER(0, 0, 0, 1, true, true, 4);
    for (int s = 1; s < 29; s += 4) {
        B_ITER(s, 1, 1, 0, false, true, 4);
        B_ITER(s + 1, 2, 0, 1, false, true, 4);
        B_ITER(s + 2, 3, 1, 0, false, true, 4);
        B_ITER(s + 3, 0, 0, 1, false, true, 4);
    }
    B_ITER(29, 1, 1, 0, false, true, 4);
    u32x2 z1v[2][4];
    {
        const char* z1b = ws + WS_Z1 + ((size_t)(tt * 128 + wt * 64) * FH + ft * 64 + wf * 32) * 2;
        const unsigned zo = r * (FH * 2) + h * 8;
#pragma unroll
        for (int i = 0; i < 2; ++i)
#pragma unroll
            for (int g = 0; g < 4; ++g) asm volatile("global_load_dwordx2 %0, %1, %2 offset:%3" : "=&v"(z1v[i][g]) : "v"(zo + i * 32 * (FH * 2)), "s"(z1b), "n"(16 * g) : "memory");
    }
    const float bias0 = __uint_as_float((unsigned)bsp[tt * 128 + wt * 64 + r] << 16), bias1 = __uint_as_float((unsigned)bsp[tt * 128 + wt * 64 + 32 + r] << 16);
    B_ITER(30, 2, 0, 1, false, false, 0);
    B_ITER(31, 3, 1, 0, false, false, 0);
#pragma unroll
    for (int ks = 0; ks < 4; ++ks) { acc[0] = MFMA(zf[1][ks], a0[1][ks], acc[0]); acc[1] = MFMA(zf[1][ks], a1[1][ks], acc[1]); }
    asm volatile("" : "+v"(z1v[0][0]), "+v"(z1v[0][1]), "+v"(z1v[0][2]), "+v"(z1v[0][3]), "+v"(z1v[1][0]), "+v"(z1v[1][1]), "+v"(z1v[1][2]), "+v"(z1v[1][3]));
    lds_ptr stg = (lds_ptr)smem + B_RING;
#pragma unroll
    for (int i = 0; i < 2; ++i) {
        const float bias = i ? bias1 : bias0;
#pragma unroll
        for (int g = 0; g < 4; ++g) {
            u32x2 pk;
            pk[0] = pack2(bf_lo(z1v[i][g][0]) * (acc[i][4 * g] + bias), bf_hi(z1v[i][g][0]) * (acc[i][4 * g + 1] + bias));
            pk[1] = pack2(bf_lo(z1v[i][g][1]) * (acc[i][4 * g + 2] + bias), bf_hi(z1v[i][g][1]) * (acc[i][4 * g + 3] + bias));
            *(LDSAS u32x2*)(stg + (wt * 64 + 32 * i + r) * 144 + wf * 64 + g * 16 + h * 8) = pk;
        }
    }
    asm volatile("s_waitcnt lgkmcnt(0)" ::: "memory");
    B_PUB();
    { CFENCE(); check(*allfl, 34u); }
    {
        char* ub = ws + WS_U + ((size_t)(tt * 128 + bw * 32) * FH + ft * 64) * 2;
#pragma unroll
        for (int i = 0; i < 4; ++i) {
            const int row = i * 8 + (lane >> 3);
            const u32x4 v = *(const LDSAS u32x4*)(stg + (bw * 32 + row) * 144 + (lane & 7) * 16);
            st16_wt(ub + (size_t)row * (FH * 2) + (lane & 7) * 16, v);
        }
    }
    asm volatile("s_waitcnt lgkmcnt(0)" ::: "memory");
    B_PUB();
    wait_vm<0>();
    {
        unsigned old = 0;
        if (lane == 0) old = __hip_atomic_fetch_add(bfl + 8, 1u, __ATOMIC_RELAXED, __HIP_MEMORY_SCOPE_WORKGROUP);
        old = __builtin_amdgcn_readfirstlane(old);
        if (old == 3 && lane == 0) __hip_atomic_fetch_add((gu32*)(ws + WS_FLAG) + (FL_CU + tt) * 32, 1u, RLX_AGENT);
    }
    } else {
        *(volatile LDSAS unsigned*)(bfl + bw) = 35u;
    }
}

constexpr int F_STAGE = 16384, F_NST = 9;
template <int N> DI void wait_bar_n() { asm volatile("s_waitcnt vmcnt(%0)\n\ts_barrier" :: "n"(N) : "memory"); }
DI void final_role(const u16* __restrict__ wo, const u16* __restrict__ wout, char* __restrict__ ws, u16* __restrict__ out, lds_cptr smem, unsigned lds0, int wid, int lane) {
    const int b = blockIdx.x, xi = b & 7, j = b >> 3;
    const int tt = 4 * xi + (j & 3), nt = j >> 2;
    const int r = lane & 31, h = lane >> 5, wt = wid & 1, wn = wid >> 1;
    const char* obase = ws + WS_O + (size_t)tt * 64 * 1024;
    const char* ubase = ws + WS_U + (size_t)tt * 64 * FH * 2;
    unsigned ooff[2], uoff[2], woff[2], wooff[2];
#pragma unroll
    for (int i = 0; i < 2; ++i) {
        const int row = (wid * 2 + i) * 8 + (lane >> 3), c = lane & 7;
        const int gc = (c ^ ((row >> 1) & 7)) << 4;
        ooff[i] = row * 1024 + gc; uoff[i] = row * (FH * 2) + gc;
        woff[i] = row * 1024 + ((c ^ (((row >> 1) & 1) << 2)) << 4);
        const int c16 = row & 15, dd = (c16 & 3) + 8 * ((c16 >> 2) & 1) + 4 * (c16 >> 3);
        wooff[i] = ((row & ~15) + dd) * 1024 + ((c ^ (((row >> 1) & 1) << 2)) << 4);
    }
    auto issueW = [&](int kt) {
        const unsigned lw = lds0 + (kt % F_NST) * F_STAGE + wid * 2048 + 8192;
        if (kt < 8) { const char* wb = (const char*)wo + (size_t)kt * 65536 + nt * 128; glds16(wb, wooff[0], lw); glds16(wb, wooff[1], lw + 1024); }
        else { const char* wb = (const char*)wout + (size_t)(kt - 8) * 65536 + nt * 128; glds16(wb, woff[0], lw); glds16(wb, woff[1], lw + 1024); }
    };
    auto issueA = [&](int kt) {
        const unsigned la = lds0 + (kt % F_NST) * F_STAGE + wid * 2048;
        if (kt < 8) { const char* ab = obase + kt * 128; glds16_sc1(ab, ooff[0], la); glds16_sc1(ab, ooff[1], la + 1024); }
        else { const char* ab = ubase + (kt - 8) * 128; glds16_sc1(ab, uoff[0], la); glds16_sc1(ab, uoff[1], la + 1024); }
    };
    asm volatile("s_waitcnt vmcnt(0) lgkmcnt(0)\n\ts_barrier" ::: "memory");
#pragma unroll
    for (int k = 0; k < F_NST; ++k) issueW(k);
    const float g = ((const float*)(ws + WS_G))[tt * 64 + wt * 32 + r], g1 = 1.f - g;
    if (wid == 0) {
        gu32* fp = (gu32*)(ws + WS_FLAG) + ((lane & 1) ? FL_CU + (tt >> 1) : FL_CO + (tt >> 2)) * 32;
        const unsigned want = (lane & 1) ? 14u : 32u;
        unsigned spins = 0;
        unsigned f0 = __hip_atomic_load(fp, RLX_AGENT);
        while (true) {
            __builtin_amdgcn_s_sleep(1);
            const unsigned f1 = __hip_atomic_load(fp, RLX_AGENT);
            if (!__any(f0 != want) || ++spins > (1u << 20)) break;
            f0 = f1;
        }
    }
    asm volatile("s_barrier" ::: "memory");
#pragma unroll
    for (int k = 0; k < F_NST; ++k) issueA(k);
    const int swz = (r >> 1) & 7;
    int aa[4];
#pragma unroll
    for (int ks = 0; ks < 4; ++ks) aa[ks] = (wt * 32 + r) * 128 + (((2 * ks + h) ^ swz) << 4);
    const int i16 = lane & 15, q = i16 >> 2, pp = i16 & 3;
    const int wch = (4 * wn + 2 * ((lane >> 4) & 1) + (pp >> 1)) ^ (((q >> 1) & 1) << 2);
    const int wa = 8192 + (8 * h + q) * 128 + wch * 16 + (pp & 1) * 8;
    f32x16 accA = f32x16{}, accB = f32x16{};
    auto step = [&](auto KT) {
        constexpr int kt = decltype(KT)::value;
        constexpr int young = kt <= 8 ? 2 * (8 - kt) + 4 * (kt > 1 ? kt - 1 : 0) : 4 * (21 - kt < 7 ? 21 - kt : 7);
        wait_bar_n<young>();
        lds_cptr sb = smem + (kt % F_NST) * F_STAGE;
        bf16x8 wf[4], af[4];
#pragma unroll
        for (int ks = 0; ks < 4; ++ks) { wf[ks] = lds_tr2(sb + wa + ks * 2048, 512); af[ks] = lds_b128(sb + aa[ks]); }
        if (kt >= 1 && kt + 8 < 22) { issueA(kt + 8); issueW(kt + 8); }
#pragma unroll
        for (int ks = 0; ks < 4; ++ks) { if (kt < 8) accA = MFMA(wf[ks], af[ks], accA); else accB = MFMA(wf[ks], af[ks], accB); }
    };
#define FS(k) step(std::integral_constant<int, k>{})
    FS(0); FS(1); FS(2); FS(3); FS(4); FS(5); FS(6); FS(7); FS(8); FS(9); FS(10); FS(11); FS(12); FS(13); FS(14); FS(15); FS(16); FS(17); FS(18); FS(19); FS(20); FS(21);
#undef FS
    lds_ptr stg = (lds_ptr)smem + (20 % F_NST) * F_STAGE;
#pragma unroll
    for (int gg = 0; gg < 4; ++gg) {
        u32x2 pk;
        pk[0] = pack2(g * accA[4 * gg] + g1 * accB[4 * gg], g * accA[4 * gg + 1] + g1 * accB[4 * gg + 1]);
        pk[1] = pack2(g * accA[4 * gg + 2] + g1 * accB[4 * gg + 2], g * accA[4 * gg + 3] + g1 * accB[4 * gg + 3]);
        *(LDSAS u32x2*)(stg + (wt * 32 + r) * 144 + wn * 64 + gg * 16 + h * 8) = pk;
    }
    asm volatile("s_waitcnt lgkmcnt(0)\n\ts_barrier" ::: "memory");
#pragma unroll
    for (int i = 0; i < 2; ++i) {
        const int row = wid * 16 + i * 8 + (lane >> 3);
        const u32x4 v = *(const LDSAS u32x4*)(stg + row * 144 + (lane & 7) * 16);
        st16_wt((char*)out + ((size_t)(tt * 64 + row) * D_ + nt * 64) * 2 + (lane & 7) * 16, v);
    }
}

__global__ __launch_bounds__(768, 3) void k2_kernel(const u16* __restrict__ wsp, const u16* __restrict__ bsp, const u16* __restrict__ wo, const u16* __restrict__ wout,
        char* __restrict__ ws, u16* __restrict__ out) {
    extern __shared__ __attribute__((aligned(16))) char smem_[];
    lds_cptr smem = (lds_cptr)smem_;
    const unsigned lds0 = (unsigned)(size_t)smem;
    const int tid = threadIdx.x, lane = tid & 63, wid = __builtin_amdgcn_readfirstlane(tid >> 6);
    if (tid < 32) *(LDSAS unsigned*)((lds_ptr)smem_ + AT_FLAGS + tid * 4) = 0;
    __syncthreads();
    if (wid < 8) {
        attn_role(ws, ws, smem, lds0, wid, lane);
    } else {
        __builtin_amdgcn_s_setprio(B_PRIO);
        b_role(wsp, bsp, ws, smem, lds0, wid - 8, lane);
        __builtin_amdgcn_s_setprio(0);
        final_role(wo, wout, ws, out, smem, lds0, wid - 8, lane);
    }
}

extern "C" void kernel_launch(void* const* d_in, const int* in_sizes, int n_in, void* d_out, int out_size, void* d_ws, size_t ws_size, hipStream_t stream) {
    const u16* x = (const u16*)d_in[0];
    const u16* wqkv = (const u16*)d_in[1];
    const u16* wo = (const u16*)d_in[2];
    const u16* win = (const u16*)d_in[4];
    const u16* wsp = (const u16*)d_in[5];
    const u16* bsp = (const u16*)d_in[6];
    const u16* wout = (const u16*)d_in[7];
    const u16* gw = (const u16*)d_in[8];
    char* ws = (char*)d_ws;
    (void)hipFuncSetAttribute((const void*)k1_kernel, hipFuncAttributeMaxDynamicSharedMemorySize, K1_LDS2);
    (void)hipFuncSetAttribute((const void*)k2_kernel, hipFuncAttributeMaxDynamicSharedMemorySize, K2_LDS);
    k1_kernel<<<256, 768, K1_LDS2, stream>>>(x, wqkv, win, gw, wsp, wo, wout, ws);
    k2_kernel<<<256, 768, K2_LDS, stream>>>(wsp, bsp, wo, wout, ws, (u16*)d_out);
}
```
